```python
import torch

def _softmax(x, axis=-1):
    return torch.softmax(x, dim=axis)
def _gelu(x):
    return torch.nn.functional.gelu(x, approximate="tanh")
def _silu(x):
    return torch.nn.functional.silu(x)
def _sigmoid(x):
    return torch.sigmoid(x)
def _layernorm(x, axis=-1, eps=1e-5):
    m = x.mean(dim=axis, keepdim=True)
    v = x.var(dim=axis, keepdim=True, unbiased=False)
    return (x - m) / torch.sqrt(v + eps)
def _scatter_add(tgt, idx, src, axis=0):
    return tgt.clone().index_add_(axis, idx, src)
def _segment_sum(src, seg, n):
    out = torch.zeros((n,) + src.shape[1:], dtype=src.dtype)
    return out.index_add_(0, seg, src)
def _pad(x, pw):
    flat = tuple(v for pair in reversed(pw) for v in pair)
    return torch.nn.functional.pad(x, flat)

kernel_name = 'standalone__w4a16_gemm_dequant__prompt'

def setup_inputs():

    _w = (lambda a: (lambda t: t.to(torch.bfloat16) if t.is_floating_point() else t)(torch.as_tensor(a)))
    B, S, D, F = 1, 2048, 576, 4608
    G = -1 if -1 > 0 else D
    D = (D // G) * G
    ng = D // G
    qw = torch.randint(0, 16, (D, F)).to(torch.int32)
    return {
        "x": _w((lambda *s: torch.randn(*s, dtype=torch.bfloat16) * 0.05)(B, S, D)),
        "qw": _w(qw),
        "scales": _w(torch.abs((lambda *s: torch.randn(*s, dtype=torch.bfloat16) * 0.05)(ng, F)) + 0.01),
        "zeros": _w(torch.full((ng, F), 8.0, dtype=float)),
    }

def reference(x, qw, scales, zeros):
    B, S, D = x.shape
    ng, F = scales.shape
    G = D // ng
    s = (lambda x, n, axis: torch.repeat_interleave(x, n, dim=axis))(scales, G, axis=0)
    z = (lambda x, n, axis: torch.repeat_interleave(x, n, dim=axis))(zeros, G, axis=0)
    w = ((lambda x, dt: x.to(dt))(qw, x.dtype) - z) * s
    return x @ w
```

```cpp
#include <hip/hip_runtime.h>
#include <hip/hip_bf16.h>
#include <stdint.h>

typedef __attribute__((ext_vector_type(8))) short bf16x8;
typedef __attribute__((ext_vector_type(4))) float f32x4;
typedef __attribute__((ext_vector_type(2))) __bf16 bf16x2_t;
typedef __attribute__((ext_vector_type(2))) float f32x2;

constexpr int M_ = 2048, K_ = 576, N_ = 4608;

__device__ __forceinline__ unsigned pack_bf16(float a, float b) {
  f32x2 v = {a, b};
  bf16x2_t r = __builtin_convertvector(v, bf16x2_t);
  return __builtin_bit_cast(unsigned, r);
}
__device__ __forceinline__ float bf2f(unsigned short u) { return __builtin_bit_cast(float, (unsigned)u << 16); }

__global__ __launch_bounds__(256) void k_v0(const unsigned short* __restrict__ x, const int* __restrict__ qw,
                                            const unsigned short* __restrict__ sc, const unsigned short* __restrict__ zr,
                                            unsigned short* __restrict__ out) {
  int lane = threadIdx.x & 63, w = threadIdx.x >> 6;
  int nb = blockIdx.x % (N_ / 128), mb = blockIdx.x / (N_ / 128);
  int n0 = nb * 128 + w * 32, m0 = mb * 64;
  int r = lane & 15, kq = lane >> 4;
  float s0 = bf2f(sc[n0 + 2 * r]), s1 = bf2f(sc[n0 + 2 * r + 1]);
  float z0 = bf2f(zr[n0 + 2 * r]), z1 = bf2f(zr[n0 + 2 * r + 1]);
  float c0 = -z0 * s0, c1 = -z1 * s1;
  f32x4 acc[4][2] = {};
  for (int ks = 0; ks < K_ / 32; ++ks) {
    int2 q[8];
#pragma unroll
    for (int j = 0; j < 8; ++j) q[j] = *(const int2*)(qw + (size_t)(ks * 32 + kq * 8 + j) * N_ + n0 + 2 * r);
    unsigned a0[4], a1[4];
#pragma unroll
    for (int j = 0; j < 4; ++j) {
      a0[j] = pack_bf16(__builtin_fmaf((float)q[2 * j].x, s0, c0), __builtin_fmaf((float)q[2 * j + 1].x, s0, c0));
      a1[j] = pack_bf16(__builtin_fmaf((float)q[2 * j].y, s1, c1), __builtin_fmaf((float)q[2 * j + 1].y, s1, c1));
    }
    bf16x8 A0 = __builtin_bit_cast(bf16x8, *(uint4*)a0), A1 = __builtin_bit_cast(bf16x8, *(uint4*)a1);
#pragma unroll
    for (int mt = 0; mt < 4; ++mt) {
      bf16x8 B = *(const bf16x8*)(x + (size_t)(m0 + mt * 16 + r) * K_ + ks * 32 + kq * 8);
      acc[mt][0] = __builtin_amdgcn_mfma_f32_16x16x32_bf16(A0, B, acc[mt][0], 0, 0, 0);
      acc[mt][1] = __builtin_amdgcn_mfma_f32_16x16x32_bf16(A1, B, acc[mt][1], 0, 0, 0);
    }
  }
#pragma unroll
  for (int mt = 0; mt < 4; ++mt) {
    uint4 o;
    o.x = pack_bf16(acc[mt][0][0], acc[mt][1][0]);
    o.y = pack_bf16(acc[mt][0][1], acc[mt][1][1]);
    o.z = pack_bf16(acc[mt][0][2], acc[mt][1][2]);
    o.w = pack_bf16(acc[mt][0][3], acc[mt][1][3]);
    *(uint4*)(out + (size_t)(m0 + mt * 16 + r) * N_ + n0 + kq * 8) = o;
  }
}

extern "C" void kernel_launch(void* const* d_in, const int* in_sizes, int n_in, void* d_out, int out_size, void* d_ws,
                              size_t ws_size, hipStream_t stream) {
  k_v0<<<(N_ / 128) * (M_ / 64), 256, 0, stream>>>((const unsigned short*)d_in[0], (const int*)d_in[1],
                                                   (const unsigned short*)d_in[2], (const unsigned short*)d_in[3],
                                                   (unsigned short*)d_out);
}
```

```cpp
#include <hip/hip_runtime.h>
#include <hip/hip_bf16.h>
#include <stdint.h>

__global__ __launch_bounds__(512, 2) void w4a16_gemm_kernel(const void* __restrict__ x, const void* __restrict__ qw,
                                                           const void* __restrict__ sc, const void* __restrict__ zr,
                                                           void* __restrict__ out) {
  __shared__ __attribute__((aligned(1024))) char smem[159744];
  const unsigned lds = (unsigned)(size_t)(__attribute__((address_space(3))) char*)smem;
  const uint64_t ka = (uint64_t)__builtin_amdgcn_kernarg_segment_ptr();
  const unsigned bid = blockIdx.x;
  unsigned tid = threadIdx.x;
  asm volatile(
      "s_load_dwordx2 s[20:21], %[ka], 0\n\t"
      "s_load_dwordx2 s[24:25], %[ka], 8\n\t"
      "s_load_dwordx2 s[28:29], %[ka], 16\n\t"
      "s_load_dwordx2 s[4:5], %[ka], 24\n\t"
      "s_load_dwordx2 s[36:37], %[ka], 32\n\t"
      "v_lshrrev_b32 v11, 6, v0\n\t"
      "s_and_b32 s50, %[bid], 7\n\t"
      "v_readfirstlane_b32 s8, v11\n\t"
      "s_lshr_b32 s11, %[bid], 6\n\t"
      "s_bfe_u32 s10, %[bid], 0x30003\n\t"
      "s_mul_i32 s13, s50, 576\n\t"
      "s_lshl_b32 s50, s11, 7\n\t"
      "s_add_u32 s12, s13, s50\n\t"
      "s_add_u32 s50, s10, s11\n\t"
      "s_mul_i32 s51, s50, 11\n\t"
      "s_lshr_b32 s51, s51, 5\n\t"
      "s_mul_i32 s51, s51, 3\n\t"
      "s_sub_u32 s50, s50, s51\n\t"
      "s_lshl_b32 s40, s50, 7\n\t"
      "s_and_b32 s9, s8, 3\n\t"
      "s_cmp_lt_u32 s8, 4\n\t"
      "s_cbranch_scc0 LC_%=\n\t"
      "v_bfe_u32 v11, v0, 4, 2\n\t"
      "v_and_b32 v12, 15, v0\n\t"
      "v_mul_u32_u24 v3, 147456, v11\n\t"
      "v_lshl_add_u32 v3, v12, 4, v3\n\t"
      "s_lshl_b32 s50, s12, 2\n\t"
      "v_add_u32 v3, s50, v3\n\t"
      "s_and_b32 s15, s9, 1\n\t"
      "s_mul_i32 s15, s15, 589824\n\t"
      "s_lshr_b32 s53, s9, 1\n\t"
      "s_lshl_b32 s50, s53, 8\n\t"
      "s_add_u32 s16, s15, s50\n\t"
      "s_waitcnt lgkmcnt(0)\n\t"
      "s_and_b32 s25, s25, 0xffff\n\t"
      "s_mov_b32 s26, 10616832\n\t"
      "s_mov_b32 s27, 0x20000\n\t"
      "s_mul_i32 s70, s40, 9216\n\t"
      "s_add_u32 s70, s70, s16\n\t"
      "buffer_load_dwordx4 v[32:35], v3, s[24:27], s70 offen\n\t"
      "s_add_u32 s70, s70, 18432\n\t"
      "buffer_load_dwordx4 v[36:39], v3, s[24:27], s70 offen\n\t"
      "s_add_u32 s70, s70, 18432\n\t"
      "buffer_load_dwordx4 v[40:43], v3, s[24:27], s70 offen\n\t"
      "s_add_u32 s70, s70, 18432\n\t"
      "buffer_load_dwordx4 v[44:47], v3, s[24:27], s70 offen\n\t"
      "s_add_u32 s70, s70, 18432\n\t"
      "buffer_load_dwordx4 v[48:51], v3, s[24:27], s70 offen\n\t"
      "s_add_u32 s70, s70, 18432\n\t"
      "buffer_load_dwordx4 v[52:55], v3, s[24:27], s70 offen\n\t"
      "s_add_u32 s70, s70, 18432\n\t"
      "buffer_load_dwordx4 v[56:59], v3, s[24:27], s70 offen\n\t"
      "s_add_u32 s70, s70, 18432\n\t"
      "buffer_load_dwordx4 v[60:63], v3, s[24:27], s70 offen\n\t"
      "s_and_b32 s29, s29, 0xffff\n\t"
      "s_mov_b32 s30, 9216\n\t"
      "s_mov_b32 s31, 0x20000\n\t"
      "s_and_b32 s5, s5, 0xffff\n\t"
      "s_mov_b32 s6, 9216\n\t"
      "s_mov_b32 s7, 0x20000\n\t"
      "s_lshl_b32 s50, s12, 1\n\t"
      "v_lshlrev_b32 v7, 3, v12\n\t"
      "v_add_u32 v7, s50, v7\n\t"
      "s_lshl_b32 s54, s53, 7\n\t"
      "buffer_load_dwordx2 v[192:193], v7, s[28:31], s54 offen\n\t"
      "buffer_load_dwordx2 v[194:195], v7, s[4:7], s54 offen\n\t"
      "s_and_b32 s50, s11, 1\n\t"
      "s_lshl_b32 s50, s50, 5\n\t"
      "s_add_u32 s13, s13, s50\n\t"
      "s_add_u32 s13, s13, 512\n\t"
      "v_bfe_u32 v11, v0, 3, 3\n\t"
      "v_and_b32 v13, 7, v0\n\t"
      "v_mul_u32_u24 v5, 147456, v11\n\t"
      "v_lshl_add_u32 v5, v13, 4, v5\n\t"
      "s_mul_i32 s50, s9, 36864\n\t"
      "s_lshl_b32 s51, s13, 2\n\t"
      "s_add_u32 s50, s50, s51\n\t"
      "v_add_u32 v5, s50, v5\n\t"
      "s_mul_i32 s71, s40, 9216\n\t"
      "buffer_load_dwordx4 v[160:163], v5, s[24:27], s71 offen\n\t"
      "s_add_u32 s71, s71, 18432\n\t"
      "buffer_load_dwordx4 v[164:167], v5, s[24:27], s71 offen\n\t"
      "s_lshl_b32 s50, s13, 1\n\t"
      "v_lshlrev_b32 v8, 3, v13\n\t"
      "v_add_u32 v8, s50, v8\n\t"
      "buffer_load_dwordx2 v[196:197], v8, s[28:31], 0 offen\n\t"
      "buffer_load_dwordx2 v[198:199], v8, s[4:7], 0 offen\n\t"
      "s_add_u32 s50, s40, 128\n\t"
      "s_cmp_ge_u32 s50, 1152\n\t"
      "s_cselect_b32 s41, 0, s50\n\t"
      "s_mul_i32 s70, s41, 9216\n\t"
      "s_add_u32 s70, s70, s16\n\t"
      "buffer_load_dwordx4 v[64:67], v3, s[24:27], s70 offen\n\t"
      "s_add_u32 s70, s70, 18432\n\t"
      "buffer_load_dwordx4 v[68:71], v3, s[24:27], s70 offen\n\t"
      "s_add_u32 s70, s70, 18432\n\t"
      "buffer_load_dwordx4 v[72:75], v3, s[24:27], s70 offen\n\t"
      "s_add_u32 s70, s70, 18432\n\t"
      "buffer_load_dwordx4 v[76:79], v3, s[24:27], s70 offen\n\t"
      "s_add_u32 s70, s70, 18432\n\t"
      "buffer_load_dwordx4 v[80:83], v3, s[24:27], s70 offen\n\t"
      "s_add_u32 s70, s70, 18432\n\t"
      "buffer_load_dwordx4 v[84:87], v3, s[24:27], s70 offen\n\t"
      "s_add_u32 s70, s70, 18432\n\t"
      "buffer_load_dwordx4 v[88:91], v3, s[24:27], s70 offen\n\t"
      "s_add_u32 s70, s70, 18432\n\t"
      "buffer_load_dwordx4 v[92:95], v3, s[24:27], s70 offen\n\t"
      "s_mul_i32 s71, s41, 9216\n\t"
      "buffer_load_dwordx4 v[168:171], v5, s[24:27], s71 offen\n\t"
      "s_add_u32 s71, s71, 18432\n\t"
      "buffer_load_dwordx4 v[172:175], v5, s[24:27], s71 offen\n\t"
      "v_and_b32 v4, 63, v0\n\t"
      "v_lshlrev_b32 v4, 4, v4\n\t"
      "s_and_b32 s50, s9, 1\n\t"
      "s_lshl_b32 s50, s50, 13\n\t"
      "s_lshl_b32 s51, s53, 12\n\t"
      "s_add_u32 s50, s50, s51\n\t"
      "s_add_u32 s50, s50, %[lds]\n\t"
      "v_add_u32 v4, s50, v4\n\t"
      "v_lshrrev_b32 v6, 2, v11\n\t"
      "v_lshlrev_b32 v6, 11, v6\n\t"
      "v_and_b32 v11, 3, v11\n\t"
      "v_lshl_add_u32 v6, v11, 8, v6\n\t"
      "v_lshl_add_u32 v6, v13, 4, v6\n\t"
      "s_lshl_b32 s50, s9, 2\n\t"
      "s_add_u32 s50, s50, %[lds]\n\t"
      "v_add_u32 v6, s50, v6\n\t"
      "v_add_u32 v225, 18432, v3\n\t"
      "v_add_u32 v226, 36864, v3\n\t"
      "v_add_u32 v227, 55296, v3\n\t"
      "v_add_u32 v228, 73728, v3\n\t"
      "v_add_u32 v229, 92160, v3\n\t"
      "v_add_u32 v230, 110592, v3\n\t"
      "v_add_u32 v231, 129024, v3\n\t"
      "v_add_u32 v240, 18432, v5\n\t"
      "s_and_b32 s21, s21, 0xffff\n\t"
      "s_mov_b32 s22, 2359296\n\t"
      "s_mov_b32 s23, 0x20000\n\t"
      "s_lshl_b32 s17, s10, 8\n\t"
      "s_lshl_b32 s50, s9, 5\n\t"
      "s_add_u32 s17, s17, s50\n\t"
      "s_and_b32 s50, %[bid], 0x80\n\t"
      "s_xor_b32 s51, s50, 0x80\n\t"
      "s_add_u32 s18, s17, s51\n\t"
      "s_add_u32 s17, s17, s50\n\t"
      "v_bfe_u32 v11, v0, 3, 3\n\t"
      "v_xor_b32 v12, v11, v0\n\t"
      "v_and_b32 v12, 7, v12\n\t"
      "v_mul_u32_u24 v11, 1152, v11\n\t"
      "v_lshl_add_u32 v11, v12, 4, v11\n\t"
      "s_mul_i32 s50, s17, 1152\n\t"
      "s_mul_i32 s51, s18, 1152\n\t"
      "v_add_u32 v1, s50, v11\n\t"
      "v_add_u32 v2, s51, v11\n\t"
      "s_lshl_b32 s14, s9, 13\n\t"
      "s_add_u32 s14, s14, %[lds]\n\t"
      "s_add_u32 s14, s14, 61440\n\t"
      "v_add_u32 v233, 8192, v1\n\t"
      "v_add_u32 v234, 16384, v1\n\t"
      "v_add_u32 v235, 24576, v1\n\t"
      "v_add_u32 v237, 8192, v2\n\t"
      "v_add_u32 v238, 16384, v2\n\t"
      "v_add_u32 v239, 24576, v2\n\t"
      "s_add_u32 s50, s41, 128\n\t"
      "s_cmp_ge_u32 s50, 1152\n\t"
      "s_cselect_b32 s42, 0, s50\n\t"
      "s_add_u32 s50, s42, 128\n\t"
      "s_cmp_ge_u32 s50, 1152\n\t"
      "s_cselect_b32 s43, 0, s50\n\t"
      "s_add_u32 s50, s43, 128\n\t"
      "s_cmp_ge_u32 s50, 1152\n\t"
      "s_cselect_b32 s44, 0, s50\n\t"
      "s_add_u32 s50, s44, 128\n\t"
      "s_cmp_ge_u32 s50, 1152\n\t"
      "s_cselect_b32 s45, 0, s50\n\t"
      "s_add_u32 s50, s45, 128\n\t"
      "s_cmp_ge_u32 s50, 1152\n\t"
      "s_cselect_b32 s46, 0, s50\n\t"
      "s_add_u32 s50, s46, 128\n\t"
      "s_cmp_ge_u32 s50, 1152\n\t"
      "s_cselect_b32 s47, 0, s50\n\t"
      "s_add_u32 s50, s47, 128\n\t"
      "s_cmp_ge_u32 s50, 1152\n\t"
      "s_cselect_b32 s48, 0, s50\n\t"
      "s_waitcnt vmcnt(15)\n\t"
      "v_lshlrev_b32 v16, 16, v192\n\t"
      "s_waitcnt vmcnt(14)\n\t"
      "v_lshlrev_b32 v20, 16, v194\n\t"
      "v_mul_f32 v20, -v20, v16\n\t"
      "v_and_b32 v17, 0xffff0000, v192\n\t"
      "v_and_b32 v21, 0xffff0000, v194\n\t"
      "v_mul_f32 v21, -v21, v17\n\t"
      "v_lshlrev_b32 v18, 16, v193\n\t"
      "v_lshlrev_b32 v22, 16, v195\n\t"
      "v_mul_f32 v22, -v22, v18\n\t"
      "v_and_b32 v19, 0xffff0000, v193\n\t"
      "v_and_b32 v23, 0xffff0000, v195\n\t"
      "v_mul_f32 v23, -v23, v19\n\t"
      "s_waitcnt vmcnt(11)\n\t"
      "v_lshlrev_b32 v24, 16, v196\n\t"
      "s_waitcnt vmcnt(10)\n\t"
      "v_lshlrev_b32 v28, 16, v198\n\t"
      "v_mul_f32 v28, -v28, v24\n\t"
      "v_and_b32 v25, 0xffff0000, v196\n\t"
      "v_and_b32 v29, 0xffff0000, v198\n\t"
      "v_mul_f32 v29, -v29, v25\n\t"
      "v_lshlrev_b32 v26, 16, v197\n\t"
      "v_lshlrev_b32 v30, 16, v199\n\t"
      "v_mul_f32 v30, -v30, v26\n\t"
      "v_and_b32 v27, 0xffff0000, v197\n\t"
      "v_and_b32 v31, 0xffff0000, v199\n\t"
      "v_mul_f32 v31, -v31, v27\n\t"
      "v_mov_b32 v241, 0\n\t"
      "v_cvt_f32_i32 v32, v32\n\t"
      "v_cvt_f32_i32 v33, v33\n\t"
      "v_fma_f32 v32, v32, v16, v20\n\t"
      "v_fma_f32 v33, v33, v17, v21\n\t"
      "v_cvt_f32_i32 v36, v36\n\t"
      "v_cvt_f32_i32 v37, v37\n\t"
      "v_fma_f32 v36, v36, v16, v20\n\t"
      "v_fma_f32 v37, v37, v17, v21\n\t"
      "v_cvt_pk_bf16_f32 v192, v32, v36\n\t"
      "v_cvt_pk_bf16_f32 v196, v33, v37\n\t"
      "v_cvt_f32_i32 v40, v40\n\t"
      "v_cvt_f32_i32 v41, v41\n\t"
      "v_fma_f32 v40, v40, v16, v20\n\t"
      "v_fma_f32 v41, v41, v17, v21\n\t"
      "v_cvt_f32_i32 v44, v44\n\t"
      "v_cvt_f32_i32 v45, v45\n\t"
      "v_fma_f32 v44, v44, v16, v20\n\t"
      "v_fma_f32 v45, v45, v17, v21\n\t"
      "v_cvt_pk_bf16_f32 v193, v40, v44\n\t"
      "v_cvt_pk_bf16_f32 v197, v41, v45\n\t"
      "v_cvt_f32_i32 v48, v48\n\t"
      "v_cvt_f32_i32 v49, v49\n\t"
      "v_fma_f32 v48, v48, v16, v20\n\t"
      "v_fma_f32 v49, v49, v17, v21\n\t"
      "v_cvt_f32_i32 v52, v52\n\t"
      "v_cvt_f32_i32 v53, v53\n\t"
      "v_fma_f32 v52, v52, v16, v20\n\t"
      "v_fma_f32 v53, v53, v17, v21\n\t"
      "v_cvt_pk_bf16_f32 v194, v48, v52\n\t"
      "v_cvt_pk_bf16_f32 v198, v49, v53\n\t"
      "v_cvt_f32_i32 v56, v56\n\t"
      "v_cvt_f32_i32 v57, v57\n\t"
      "v_fma_f32 v56, v56, v16, v20\n\t"
      "v_fma_f32 v57, v57, v17, v21\n\t"
      "v_cvt_f32_i32 v60, v60\n\t"
      "v_cvt_f32_i32 v61, v61\n\t"
      "v_fma_f32 v60, v60, v16, v20\n\t"
      "v_fma_f32 v61, v61, v17, v21\n\t"
      "v_cvt_pk_bf16_f32 v195, v56, v60\n\t"
      "v_cvt_pk_bf16_f32 v199, v57, v61\n\t"
      "ds_write_b128 v4, v[192:195] offset:0\n\t"
      "ds_write_b128 v4, v[196:199] offset:1024\n\t"
      "v_cvt_f32_i32 v34, v34\n\t"
      "v_cvt_f32_i32 v35, v35\n\t"
      "v_fma_f32 v34, v34, v18, v22\n\t"
      "v_fma_f32 v35, v35, v19, v23\n\t"
      "v_cvt_f32_i32 v38, v38\n\t"
      "v_cvt_f32_i32 v39, v39\n\t"
      "v_fma_f32 v38, v38, v18, v22\n\t"
      "v_fma_f32 v39, v39, v19, v23\n\t"
      "v_cvt_pk_bf16_f32 v192, v34, v38\n\t"
      "v_cvt_pk_bf16_f32 v196, v35, v39\n\t"
      "v_cvt_f32_i32 v42, v42\n\t"
      "v_cvt_f32_i32 v43, v43\n\t"
      "v_fma_f32 v42, v42, v18, v22\n\t"
      "v_fma_f32 v43, v43, v19, v23\n\t"
      "v_cvt_f32_i32 v46, v46\n\t"
      "v_cvt_f32_i32 v47, v47\n\t"
      "v_fma_f32 v46, v46, v18, v22\n\t"
      "v_fma_f32 v47, v47, v19, v23\n\t"
      "v_cvt_pk_bf16_f32 v193, v42, v46\n\t"
      "v_cvt_pk_bf16_f32 v197, v43, v47\n\t"
      "v_cvt_f32_i32 v50, v50\n\t"
      "v_cvt_f32_i32 v51, v51\n\t"
      "v_fma_f32 v50, v50, v18, v22\n\t"
      "v_fma_f32 v51, v51, v19, v23\n\t"
      "v_cvt_f32_i32 v54, v54\n\t"
      "v_cvt_f32_i32 v55, v55\n\t"
      "v_fma_f32 v54, v54, v18, v22\n\t"
      "v_fma_f32 v55, v55, v19, v23\n\t"
      "v_cvt_pk_bf16_f32 v194, v50, v54\n\t"
      "v_cvt_pk_bf16_f32 v198, v51, v55\n\t"
      "v_cvt_f32_i32 v58, v58\n\t"
      "v_cvt_f32_i32 v59, v59\n\t"
      "v_fma_f32 v58, v58, v18, v22\n\t"
      "v_fma_f32 v59, v59, v19, v23\n\t"
      "v_cvt_f32_i32 v62, v62\n\t"
      "v_cvt_f32_i32 v63, v63\n\t"
      "v_fma_f32 v62, v62, v18, v22\n\t"
      "v_fma_f32 v63, v63, v19, v23\n\t"
      "v_cvt_pk_bf16_f32 v195, v58, v62\n\t"
      "v_cvt_pk_bf16_f32 v199, v59, v63\n\t"
      "ds_write_b128 v4, v[192:195] offset:2048\n\t"
      "ds_write_b128 v4, v[196:199] offset:3072\n\t"
      "v_cvt_f32_i32 v160, v160\n\t"
      "v_cvt_f32_i32 v161, v161\n\t"
      "v_fma_f32 v160, v160, v24, v28\n\t"
      "v_fma_f32 v161, v161, v25, v29\n\t"
      "v_cvt_f32_i32 v164, v164\n\t"
      "v_cvt_f32_i32 v165, v165\n\t"
      "v_fma_f32 v164, v164, v24, v28\n\t"
      "v_fma_f32 v165, v165, v25, v29\n\t"
      "v_cvt_pk_bf16_f32 v160, v160, v164\n\t"
      "ds_write_b32 v6, v160 offset:16384\n\t"
      "v_cvt_pk_bf16_f32 v161, v161, v165\n\t"
      "ds_write_b32 v6, v161 offset:17408\n\t"
      "v_cvt_f32_i32 v162, v162\n\t"
      "v_cvt_f32_i32 v163, v163\n\t"
      "v_fma_f32 v162, v162, v26, v30\n\t"
      "v_fma_f32 v163, v163, v27, v31\n\t"
      "v_cvt_f32_i32 v166, v166\n\t"
      "v_cvt_f32_i32 v167, v167\n\t"
      "v_fma_f32 v166, v166, v26, v30\n\t"
      "v_fma_f32 v167, v167, v27, v31\n\t"
      "v_cvt_pk_bf16_f32 v162, v162, v166\n\t"
      "ds_write_b32 v6, v162 offset:16512\n\t"
      "v_cvt_pk_bf16_f32 v163, v163, v167\n\t"
      "ds_write_b32 v6, v163 offset:17536\n\t"
      "s_waitcnt lgkmcnt(0)\n\t"
      "s_barrier\n\t"
      "s_mul_i32 s70, s42, 9216\n\t"
      "s_add_u32 s70, s70, s16\n\t"
      "buffer_load_dwordx4 v[96:99], v3, s[24:27], s70 offen\n\t"
      "buffer_load_dwordx4 v[100:103], v225, s[24:27], s70 offen\n\t"
      "buffer_load_dwordx4 v[104:107], v226, s[24:27], s70 offen\n\t"
      "buffer_load_dwordx4 v[108:111], v227, s[24:27], s70 offen\n\t"
      "buffer_load_dwordx4 v[112:115], v228, s[24:27], s70 offen\n\t"
      "buffer_load_dwordx4 v[116:119], v229, s[24:27], s70 offen\n\t"
      "buffer_load_dwordx4 v[120:123], v230, s[24:27], s70 offen\n\t"
      "buffer_load_dwordx4 v[124:127], v231, s[24:27], s70 offen\n\t"
      "s_mul_i32 s71, s42, 9216\n\t"
      "buffer_load_dwordx4 v[176:179], v5, s[24:27], s71 offen\n\t"
      "buffer_load_dwordx4 v[180:183], v240, s[24:27], s71 offen\n\t"
      "s_waitcnt vmcnt(19)\n\t"
      "v_cvt_f32_i32 v64, v64\n\t"
      "v_cvt_f32_i32 v65, v65\n\t"
      "v_fma_f32 v64, v64, v16, v20\n\t"
      "s_add_u32 m0, s14, 65536\n\t"
      "v_fma_f32 v65, v65, v17, v21\n\t"
      "s_waitcnt vmcnt(18)\n\t"
      "v_cvt_f32_i32 v68, v68\n\t"
      "v_cvt_f32_i32 v69, v69\n\t"
      "v_fma_f32 v68, v68, v16, v20\n\t"
      "v_fma_f32 v69, v69, v17, v21\n\t"
      "v_cvt_pk_bf16_f32 v192, v64, v68\n\t"
      "v_cvt_pk_bf16_f32 v196, v65, v69\n\t"
      "s_mul_i32 s70, s43, 9216\n\t"
      "s_add_u32 s70, s70, s16\n\t"
      "buffer_load_dwordx4 v[128:131], v3, s[24:27], s70 offen\n\t"
      "s_waitcnt vmcnt(18)\n\t"
      "v_cvt_f32_i32 v72, v72\n\t"
      "v_cvt_f32_i32 v73, v73\n\t"
      "v_fma_f32 v72, v72, v16, v20\n\t"
      "v_fma_f32 v73, v73, v17, v21\n\t"
      "s_waitcnt vmcnt(17)\n\t"
      "v_cvt_f32_i32 v76, v76\n\t"
      "v_cvt_f32_i32 v77, v77\n\t"
      "buffer_load_dwordx4 v1, s[20:23], s42 offen lds\n\t"
      "v_fma_f32 v76, v76, v16, v20\n\t"
      "v_fma_f32 v77, v77, v17, v21\n\t"
      "v_cvt_pk_bf16_f32 v193, v72, v76\n\t"
      "v_cvt_pk_bf16_f32 v197, v73, v77\n\t"
      "s_waitcnt vmcnt(17)\n\t"
      "v_cvt_f32_i32 v80, v80\n\t"
      "v_cvt_f32_i32 v81, v81\n\t"
      "buffer_load_dwordx4 v[132:135], v225, s[24:27], s70 offen\n\t"
      "v_fma_f32 v80, v80, v16, v20\n\t"
      "v_fma_f32 v81, v81, v17, v21\n\t"
      "s_waitcnt vmcnt(17)\n\t"
      "v_cvt_f32_i32 v84, v84\n\t"
      "v_cvt_f32_i32 v85, v85\n\t"
      "v_fma_f32 v84, v84, v16, v20\n\t"
      "v_fma_f32 v85, v85, v17, v21\n\t"
      "v_cvt_pk_bf16_f32 v194, v80, v84\n\t"
      "buffer_load_dwordx4 v233, s[20:23], s42 offen offset:1024 lds\n\t"
      "v_cvt_pk_bf16_f32 v198, v81, v85\n\t"
      "s_waitcnt vmcnt(17)\n\t"
      "v_cvt_f32_i32 v88, v88\n\t"
      "v_cvt_f32_i32 v89, v89\n\t"
      "v_fma_f32 v88, v88, v16, v20\n\t"
      "v_fma_f32 v89, v89, v17, v21\n\t"
      "s_waitcnt vmcnt(16)\n\t"
      "v_cvt_f32_i32 v92, v92\n\t"
      "buffer_load_dwordx4 v[136:139], v226, s[24:27], s70 offen\n\t"
      "v_cvt_f32_i32 v93, v93\n\t"
      "v_fma_f32 v92, v92, v16, v20\n\t"
      "v_fma_f32 v93, v93, v17, v21\n\t"
      "v_cvt_pk_bf16_f32 v195, v88, v92\n\t"
      "v_cvt_pk_bf16_f32 v199, v89, v93\n\t"
      "ds_write_b128 v4, v[192:195] offset:20480\n\t"
      "buffer_load_dwordx4 v234, s[20:23], s42 offen offset:2048 lds\n\t"
      "ds_write_b128 v4, v[196:199] offset:21504\n\t"
      "v_cvt_f32_i32 v66, v66\n\t"
      "v_cvt_f32_i32 v67, v67\n\t"
      "v_fma_f32 v66, v66, v18, v22\n\t"
      "v_fma_f32 v67, v67, v19, v23\n\t"
      "v_cvt_f32_i32 v70, v70\n\t"
      "v_cvt_f32_i32 v71, v71\n\t"
      "buffer_load_dwordx4 v[140:143], v227, s[24:27], s70 offen\n\t"
      "v_fma_f32 v70, v70, v18, v22\n\t"
      "v_fma_f32 v71, v71, v19, v23\n\t"
      "v_cvt_pk_bf16_f32 v192, v66, v70\n\t"
      "v_cvt_pk_bf16_f32 v196, v67, v71\n\t"
      "v_cvt_f32_i32 v74, v74\n\t"
      "v_cvt_f32_i32 v75, v75\n\t"
      "buffer_load_dwordx4 v235, s[20:23], s42 offen offset:3072 lds\n\t"
      "v_fma_f32 v74, v74, v18, v22\n\t"
      "v_fma_f32 v75, v75, v19, v23\n\t"
      "v_cvt_f32_i32 v78, v78\n\t"
      "v_cvt_f32_i32 v79, v79\n\t"
      "v_fma_f32 v78, v78, v18, v22\n\t"
      "v_fma_f32 v79, v79, v19, v23\n\t"
      "buffer_load_dwordx4 v[144:147], v228, s[24:27], s70 offen\n\t"
      "v_cvt_pk_bf16_f32 v193, v74, v78\n\t"
      "v_cvt_pk_bf16_f32 v197, v75, v79\n\t"
      "v_cvt_f32_i32 v82, v82\n\t"
      "v_cvt_f32_i32 v83, v83\n\t"
      "v_fma_f32 v82, v82, v18, v22\n\t"
      "v_fma_f32 v83, v83, v19, v23\n\t"
      "v_cvt_f32_i32 v86, v86\n\t"
      "s_add_u32 m0, s14, 69632\n\t"
      "v_cvt_f32_i32 v87, v87\n\t"
      "v_fma_f32 v86, v86, v18, v22\n\t"
      "v_fma_f32 v87, v87, v19, v23\n\t"
      "v_cvt_pk_bf16_f32 v194, v82, v86\n\t"
      "v_cvt_pk_bf16_f32 v198, v83, v87\n\t"
      "v_cvt_f32_i32 v90, v90\n\t"
      "buffer_load_dwordx4 v[148:151], v229, s[24:27], s70 offen\n\t"
      "v_cvt_f32_i32 v91, v91\n\t"
      "v_fma_f32 v90, v90, v18, v22\n\t"
      "v_fma_f32 v91, v91, v19, v23\n\t"
      "v_cvt_f32_i32 v94, v94\n\t"
      "v_cvt_f32_i32 v95, v95\n\t"
      "v_fma_f32 v94, v94, v18, v22\n\t"
      "buffer_load_dwordx4 v2, s[20:23], s42 offen lds\n\t"
      "v_fma_f32 v95, v95, v19, v23\n\t"
      "v_cvt_pk_bf16_f32 v195, v90, v94\n\t"
      "v_cvt_pk_bf16_f32 v199, v91, v95\n\t"
      "ds_write_b128 v4, v[192:195] offset:22528\n\t"
      "ds_write_b128 v4, v[196:199] offset:23552\n\t"
      "s_waitcnt vmcnt(22)\n\t"
      "v_cvt_f32_i32 v168, v168\n\t"
      "v_cvt_f32_i32 v169, v169\n\t"
      "buffer_load_dwordx4 v[152:155], v230, s[24:27], s70 offen\n\t"
      "v_fma_f32 v168, v168, v24, v28\n\t"
      "v_fma_f32 v169, v169, v25, v29\n\t"
      "s_waitcnt vmcnt(22)\n\t"
      "v_cvt_f32_i32 v172, v172\n\t"
      "v_cvt_f32_i32 v173, v173\n\t"
      "v_fma_f32 v172, v172, v24, v28\n\t"
      "v_fma_f32 v173, v173, v25, v29\n\t"
      "buffer_load_dwordx4 v237, s[20:23], s42 offen offset:1024 lds\n\t"
      "v_cvt_pk_bf16_f32 v168, v168, v172\n\t"
      "ds_write_b32 v6, v168 offset:36864\n\t"
      "v_cvt_pk_bf16_f32 v169, v169, v173\n\t"
      "ds_write_b32 v6, v169 offset:37888\n\t"
      "v_cvt_f32_i32 v170, v170\n\t"
      "v_cvt_f32_i32 v171, v171\n\t"
      "buffer_load_dwordx4 v[156:159], v231, s[24:27], s70 offen\n\t"
      "v_fma_f32 v170, v170, v26, v30\n\t"
      "v_fma_f32 v171, v171, v27, v31\n\t"
      "v_cvt_f32_i32 v174, v174\n\t"
      "v_cvt_f32_i32 v175, v175\n\t"
      "v_fma_f32 v174, v174, v26, v30\n\t"
      "v_fma_f32 v175, v175, v27, v31\n\t"
      "v_cvt_pk_bf16_f32 v170, v170, v174\n\t"
      "buffer_load_dwordx4 v238, s[20:23], s42 offen offset:2048 lds\n\t"
      "ds_write_b32 v6, v170 offset:36992\n\t"
      "v_cvt_pk_bf16_f32 v171, v171, v175\n\t"
      "ds_write_b32 v6, v171 offset:38016\n\t"
      "s_mul_i32 s71, s43, 9216\n\t"
      "buffer_load_dwordx4 v[184:187], v5, s[24:27], s71 offen\n\t"
      "buffer_load_dwordx4 v239, s[20:23], s42 offen offset:3072 lds\n\t"
      "buffer_load_dwordx4 v[188:191], v240, s[24:27], s71 offen\n\t"
      "s_waitcnt lgkmcnt(0)\n\t"
      "s_barrier\n\t"
      "s_waitcnt vmcnt(27)\n\t"
      "v_cvt_f32_i32 v96, v96\n\t"
      "v_cvt_f32_i32 v97, v97\n\t"
      "v_fma_f32 v96, v96, v16, v20\n\t"
      "s_add_u32 m0, s14, 0\n\t"
      "v_fma_f32 v97, v97, v17, v21\n\t"
      "s_waitcnt vmcnt(26)\n\t"
      "v_cvt_f32_i32 v100, v100\n\t"
      "v_cvt_f32_i32 v101, v101\n\t"
      "v_fma_f32 v100, v100, v16, v20\n\t"
      "v_fma_f32 v101, v101, v17, v21\n\t"
      "v_cvt_pk_bf16_f32 v192, v96, v100\n\t"
      "v_cvt_pk_bf16_f32 v196, v97, v101\n\t"
      "s_mul_i32 s70, s44, 9216\n\t"
      "s_add_u32 s70, s70, s16\n\t"
      "buffer_load_dwordx4 v[32:35], v3, s[24:27], s70 offen\n\t"
      "s_waitcnt vmcnt(26)\n\t"
      "v_cvt_f32_i32 v104, v104\n\t"
      "v_cvt_f32_i32 v105, v105\n\t"
      "v_fma_f32 v104, v104, v16, v20\n\t"
      "v_fma_f32 v105, v105, v17, v21\n\t"
      "s_waitcnt vmcnt(25)\n\t"
      "v_cvt_f32_i32 v108, v108\n\t"
      "v_cvt_f32_i32 v109, v109\n\t"
      "buffer_load_dwordx4 v1, s[20:23], s43 offen lds\n\t"
      "v_fma_f32 v108, v108, v16, v20\n\t"
      "v_fma_f32 v109, v109, v17, v21\n\t"
      "v_cvt_pk_bf16_f32 v193, v104, v108\n\t"
      "v_cvt_pk_bf16_f32 v197, v105, v109\n\t"
      "s_waitcnt vmcnt(25)\n\t"
      "v_cvt_f32_i32 v112, v112\n\t"
      "v_cvt_f32_i32 v113, v113\n\t"
      "buffer_load_dwordx4 v[36:39], v225, s[24:27], s70 offen\n\t"
      "v_fma_f32 v112, v112, v16, v20\n\t"
      "v_fma_f32 v113, v113, v17, v21\n\t"
      "s_waitcnt vmcnt(25)\n\t"
      "v_cvt_f32_i32 v116, v116\n\t"
      "v_cvt_f32_i32 v117, v117\n\t"
      "v_fma_f32 v116, v116, v16, v20\n\t"
      "v_fma_f32 v117, v117, v17, v21\n\t"
      "v_cvt_pk_bf16_f32 v194, v112, v116\n\t"
      "buffer_load_dwordx4 v233, s[20:23], s43 offen offset:1024 lds\n\t"
      "v_cvt_pk_bf16_f32 v198, v113, v117\n\t"
      "s_waitcnt vmcnt(25)\n\t"
      "v_cvt_f32_i32 v120, v120\n\t"
      "v_cvt_f32_i32 v121, v121\n\t"
      "v_fma_f32 v120, v120, v16, v20\n\t"
      "v_fma_f32 v121, v121, v17, v21\n\t"
      "s_waitcnt vmcnt(24)\n\t"
      "v_cvt_f32_i32 v124, v124\n\t"
      "buffer_load_dwordx4 v[40:43], v226, s[24:27], s70 offen\n\t"
      "v_cvt_f32_i32 v125, v125\n\t"
      "v_fma_f32 v124, v124, v16, v20\n\t"
      "v_fma_f32 v125, v125, v17, v21\n\t"
      "v_cvt_pk_bf16_f32 v195, v120, v124\n\t"
      "v_cvt_pk_bf16_f32 v199, v121, v125\n\t"
      "ds_write_b128 v4, v[192:195] offset:40960\n\t"
      "buffer_load_dwordx4 v234, s[20:23], s43 offen offset:2048 lds\n\t"
      "ds_write_b128 v4, v[196:199] offset:41984\n\t"
      "v_cvt_f32_i32 v98, v98\n\t"
      "v_cvt_f32_i32 v99, v99\n\t"
      "v_fma_f32 v98, v98, v18, v22\n\t"
      "v_fma_f32 v99, v99, v19, v23\n\t"
      "v_cvt_f32_i32 v102, v102\n\t"
      "v_cvt_f32_i32 v103, v103\n\t"
      "buffer_load_dwordx4 v[44:47], v227, s[24:27], s70 offen\n\t"
      "v_fma_f32 v102, v102, v18, v22\n\t"
      "v_fma_f32 v103, v103, v19, v23\n\t"
      "v_cvt_pk_bf16_f32 v192, v98, v102\n\t"
      "v_cvt_pk_bf16_f32 v196, v99, v103\n\t"
      "v_cvt_f32_i32 v106, v106\n\t"
      "v_cvt_f32_i32 v107, v107\n\t"
      "buffer_load_dwordx4 v235, s[20:23], s43 offen offset:3072 lds\n\t"
      "v_fma_f32 v106, v106, v18, v22\n\t"
      "v_fma_f32 v107, v107, v19, v23\n\t"
      "v_cvt_f32_i32 v110, v110\n\t"
      "v_cvt_f32_i32 v111, v111\n\t"
      "v_fma_f32 v110, v110, v18, v22\n\t"
      "v_fma_f32 v111, v111, v19, v23\n\t"
      "buffer_load_dwordx4 v[48:51], v228, s[24:27], s70 offen\n\t"
      "v_cvt_pk_bf16_f32 v193, v106, v110\n\t"
      "v_cvt_pk_bf16_f32 v197, v107, v111\n\t"
      "v_cvt_f32_i32 v114, v114\n\t"
      "v_cvt_f32_i32 v115, v115\n\t"
      "v_fma_f32 v114, v114, v18, v22\n\t"
      "v_fma_f32 v115, v115, v19, v23\n\t"
      "v_cvt_f32_i32 v118, v118\n\t"
      "s_add_u32 m0, s14, 4096\n\t"
      "v_cvt_f32_i32 v119, v119\n\t"
      "v_fma_f32 v118, v118, v18, v22\n\t"
      "v_fma_f32 v119, v119, v19, v23\n\t"
      "v_cvt_pk_bf16_f32 v194, v114, v118\n\t"
      "v_cvt_pk_bf16_f32 v198, v115, v119\n\t"
      "v_cvt_f32_i32 v122, v122\n\t"
      "buffer_load_dwordx4 v[52:55], v229, s[24:27], s70 offen\n\t"
      "v_cvt_f32_i32 v123, v123\n\t"
      "v_fma_f32 v122, v122, v18, v22\n\t"
      "v_fma_f32 v123, v123, v19, v23\n\t"
      "v_cvt_f32_i32 v126, v126\n\t"
      "v_cvt_f32_i32 v127, v127\n\t"
      "v_fma_f32 v126, v126, v18, v22\n\t"
      "buffer_load_dwordx4 v2, s[20:23], s43 offen lds\n\t"
      "v_fma_f32 v127, v127, v19, v23\n\t"
      "v_cvt_pk_bf16_f32 v195, v122, v126\n\t"
      "v_cvt_pk_bf16_f32 v199, v123, v127\n\t"
      "ds_write_b128 v4, v[192:195] offset:43008\n\t"
      "ds_write_b128 v4, v[196:199] offset:44032\n\t"
      "s_waitcnt vmcnt(30)\n\t"
      "v_cvt_f32_i32 v176, v176\n\t"
      "v_cvt_f32_i32 v177, v177\n\t"
      "buffer_load_dwordx4 v[56:59], v230, s[24:27], s70 offen\n\t"
      "v_fma_f32 v176, v176, v24, v28\n\t"
      "v_fma_f32 v177, v177, v25, v29\n\t"
      "s_waitcnt vmcnt(30)\n\t"
      "v_cvt_f32_i32 v180, v180\n\t"
      "v_cvt_f32_i32 v181, v181\n\t"
      "v_fma_f32 v180, v180, v24, v28\n\t"
      "v_fma_f32 v181, v181, v25, v29\n\t"
      "buffer_load_dwordx4 v237, s[20:23], s43 offen offset:1024 lds\n\t"
      "v_cvt_pk_bf16_f32 v176, v176, v180\n\t"
      "ds_write_b32 v6, v176 offset:57344\n\t"
      "v_cvt_pk_bf16_f32 v177, v177, v181\n\t"
      "ds_write_b32 v6, v177 offset:58368\n\t"
      "v_cvt_f32_i32 v178, v178\n\t"
      "v_cvt_f32_i32 v179, v179\n\t"
      "buffer_load_dwordx4 v[60:63], v231, s[24:27], s70 offen\n\t"
      "v_fma_f32 v178, v178, v26, v30\n\t"
      "v_fma_f32 v179, v179, v27, v31\n\t"
      "v_cvt_f32_i32 v182, v182\n\t"
      "v_cvt_f32_i32 v183, v183\n\t"
      "v_fma_f32 v182, v182, v26, v30\n\t"
      "v_fma_f32 v183, v183, v27, v31\n\t"
      "v_cvt_pk_bf16_f32 v178, v178, v182\n\t"
      "buffer_load_dwordx4 v238, s[20:23], s43 offen offset:2048 lds\n\t"
      "ds_write_b32 v6, v178 offset:57472\n\t"
      "v_cvt_pk_bf16_f32 v179, v179, v183\n\t"
      "ds_write_b32 v6, v179 offset:58496\n\t"
      "s_mul_i32 s71, s44, 9216\n\t"
      "buffer_load_dwordx4 v[160:163], v5, s[24:27], s71 offen\n\t"
      "buffer_load_dwordx4 v239, s[20:23], s43 offen offset:3072 lds\n\t"
      "buffer_load_dwordx4 v[164:167], v240, s[24:27], s71 offen\n\t"
      "s_waitcnt vmcnt(19)\n\t"
      "s_waitcnt lgkmcnt(0)\n\t"
      "s_barrier\n\t"
      "v_cvt_f32_i32 v128, v128\n\t"
      "v_cvt_f32_i32 v129, v129\n\t"
      "v_fma_f32 v128, v128, v16, v20\n\t"
      "s_add_u32 m0, s14, 32768\n\t"
      "v_fma_f32 v129, v129, v17, v21\n\t"
      "v_cvt_f32_i32 v132, v132\n\t"
      "v_cvt_f32_i32 v133, v133\n\t"
      "v_fma_f32 v132, v132, v16, v20\n\t"
      "v_fma_f32 v133, v133, v17, v21\n\t"
      "v_cvt_pk_bf16_f32 v192, v128, v132\n\t"
      "s_mul_i32 s70, s45, 9216\n\t"
      "s_add_u32 s70, s70, s16\n\t"
      "buffer_load_dwordx4 v[64:67], v3, s[24:27], s70 offen\n\t"
      "v_cvt_pk_bf16_f32 v196, v129, v133\n\t"
      "v_cvt_f32_i32 v136, v136\n\t"
      "v_cvt_f32_i32 v137, v137\n\t"
      "v_fma_f32 v136, v136, v16, v20\n\t"
      "v_fma_f32 v137, v137, v17, v21\n\t"
      "buffer_load_dwordx4 v1, s[20:23], s44 offen lds\n\t"
      "v_cvt_f32_i32 v140, v140\n\t"
      "v_cvt_f32_i32 v141, v141\n\t"
      "v_fma_f32 v140, v140, v16, v20\n\t"
      "v_fma_f32 v141, v141, v17, v21\n\t"
      "v_cvt_pk_bf16_f32 v193, v136, v140\n\t"
      "v_cvt_pk_bf16_f32 v197, v137, v141\n\t"
      "buffer_load_dwordx4 v[68:71], v225, s[24:27], s70 offen\n\t"
      "v_cvt_f32_i32 v144, v144\n\t"
      "v_cvt_f32_i32 v145, v145\n\t"
      "v_fma_f32 v144, v144, v16, v20\n\t"
      "v_fma_f32 v145, v145, v17, v21\n\t"
      "v_cvt_f32_i32 v148, v148\n\t"
      "v_cvt_f32_i32 v149, v149\n\t"
      "buffer_load_dwordx4 v233, s[20:23], s44 offen offset:1024 lds\n\t"
      "v_fma_f32 v148, v148, v16, v20\n\t"
      "v_fma_f32 v149, v149, v17, v21\n\t"
      "v_cvt_pk_bf16_f32 v194, v144, v148\n\t"
      "v_cvt_pk_bf16_f32 v198, v145, v149\n\t"
      "v_cvt_f32_i32 v152, v152\n\t"
      "v_cvt_f32_i32 v153, v153\n\t"
      "buffer_load_dwordx4 v[72:75], v226, s[24:27], s70 offen\n\t"
      "v_fma_f32 v152, v152, v16, v20\n\t"
      "v_fma_f32 v153, v153, v17, v21\n\t"
      "v_cvt_f32_i32 v156, v156\n\t"
      "v_cvt_f32_i32 v157, v157\n\t"
      "v_fma_f32 v156, v156, v16, v20\n\t"
      "v_fma_f32 v157, v157, v17, v21\n\t"
      "buffer_load_dwordx4 v234, s[20:23], s44 offen offset:2048 lds\n\t"
      "v_cvt_pk_bf16_f32 v195, v152, v156\n\t"
      "v_cvt_pk_bf16_f32 v199, v153, v157\n\t"
      "ds_write_b128 v4, v[192:195] offset:0\n\t"
      "ds_write_b128 v4, v[196:199] offset:1024\n\t"
      "v_cvt_f32_i32 v130, v130\n\t"
      "buffer_load_dwordx4 v[76:79], v227, s[24:27], s70 offen\n\t"
      "v_cvt_f32_i32 v131, v131\n\t"
      "v_fma_f32 v130, v130, v18, v22\n\t"
      "v_fma_f32 v131, v131, v19, v23\n\t"
      "v_cvt_f32_i32 v134, v134\n\t"
      "v_cvt_f32_i32 v135, v135\n\t"
      "v_fma_f32 v134, v134, v18, v22\n\t"
      "buffer_load_dwordx4 v235, s[20:23], s44 offen offset:3072 lds\n\t"
      "v_fma_f32 v135, v135, v19, v23\n\t"
      "v_cvt_pk_bf16_f32 v192, v130, v134\n\t"
      "v_cvt_pk_bf16_f32 v196, v131, v135\n\t"
      "v_cvt_f32_i32 v138, v138\n\t"
      "v_cvt_f32_i32 v139, v139\n\t"
      "v_fma_f32 v138, v138, v18, v22\n\t"
      "buffer_load_dwordx4 v[80:83], v228, s[24:27], s70 offen\n\t"
      "v_fma_f32 v139, v139, v19, v23\n\t"
      "v_cvt_f32_i32 v142, v142\n\t"
      "v_cvt_f32_i32 v143, v143\n\t"
      "v_fma_f32 v142, v142, v18, v22\n\t"
      "v_fma_f32 v143, v143, v19, v23\n\t"
      "v_cvt_pk_bf16_f32 v193, v138, v142\n\t"
      "s_add_u32 m0, s14, 36864\n\t"
      "v_cvt_pk_bf16_f32 v197, v139, v143\n\t"
      "v_cvt_f32_i32 v146, v146\n\t"
      "v_cvt_f32_i32 v147, v147\n\t"
      "v_fma_f32 v146, v146, v18, v22\n\t"
      "v_fma_f32 v147, v147, v19, v23\n\t"
      "buffer_load_dwordx4 v[84:87], v229, s[24:27], s70 offen\n\t"
      "v_cvt_f32_i32 v150, v150\n\t"
      "v_cvt_f32_i32 v151, v151\n\t"
      "v_fma_f32 v150, v150, v18, v22\n\t"
      "v_fma_f32 v151, v151, v19, v23\n\t"
      "v_cvt_pk_bf16_f32 v194, v146, v150\n\t"
      "v_cvt_pk_bf16_f32 v198, v147, v151\n\t"
      "buffer_load_dwordx4 v2, s[20:23], s44 offen lds\n\t"
      "v_cvt_f32_i32 v154, v154\n\t"
      "v_cvt_f32_i32 v155, v155\n\t"
      "v_fma_f32 v154, v154, v18, v22\n\t"
      "v_fma_f32 v155, v155, v19, v23\n\t"
      "v_cvt_f32_i32 v158, v158\n\t"
      "v_cvt_f32_i32 v159, v159\n\t"
      "buffer_load_dwordx4 v[88:91], v230, s[24:27], s70 offen\n\t"
      "v_fma_f32 v158, v158, v18, v22\n\t"
      "v_fma_f32 v159, v159, v19, v23\n\t"
      "v_cvt_pk_bf16_f32 v195, v154, v158\n\t"
      "v_cvt_pk_bf16_f32 v199, v155, v159\n\t"
      "ds_write_b128 v4, v[192:195] offset:2048\n\t"
      "ds_write_b128 v4, v[196:199] offset:3072\n\t"
      "buffer_load_dwordx4 v237, s[20:23], s44 offen offset:1024 lds\n\t"
      "v_cvt_f32_i32 v184, v184\n\t"
      "v_cvt_f32_i32 v185, v185\n\t"
      "v_fma_f32 v184, v184, v24, v28\n\t"
      "v_fma_f32 v185, v185, v25, v29\n\t"
      "s_waitcnt vmcnt(31)\n\t"
      "v_cvt_f32_i32 v188, v188\n\t"
      "v_cvt_f32_i32 v189, v189\n\t"
      "buffer_load_dwordx4 v[92:95], v231, s[24:27], s70 offen\n\t"
      "v_fma_f32 v188, v188, v24, v28\n\t"
      "v_fma_f32 v189, v189, v25, v29\n\t"
      "v_cvt_pk_bf16_f32 v184, v184, v188\n\t"
      "ds_write_b32 v6, v184 offset:16384\n\t"
      "v_cvt_pk_bf16_f32 v185, v185, v189\n\t"
      "buffer_load_dwordx4 v238, s[20:23], s44 offen offset:2048 lds\n\t"
      "ds_write_b32 v6, v185 offset:17408\n\t"
      "v_cvt_f32_i32 v186, v186\n\t"
      "v_cvt_f32_i32 v187, v187\n\t"
      "v_fma_f32 v186, v186, v26, v30\n\t"
      "v_fma_f32 v187, v187, v27, v31\n\t"
      "v_cvt_f32_i32 v190, v190\n\t"
      "s_mul_i32 s71, s45, 9216\n\t"
      "buffer_load_dwordx4 v[168:171], v5, s[24:27], s71 offen\n\t"
      "v_cvt_f32_i32 v191, v191\n\t"
      "v_fma_f32 v190, v190, v26, v30\n\t"
      "v_fma_f32 v191, v191, v27, v31\n\t"
      "v_cvt_pk_bf16_f32 v186, v186, v190\n\t"
      "ds_write_b32 v6, v186 offset:16512\n\t"
      "v_cvt_pk_bf16_f32 v187, v187, v191\n\t"
      "buffer_load_dwordx4 v239, s[20:23], s44 offen offset:3072 lds\n\t"
      "ds_write_b32 v6, v187 offset:17536\n\t"
      "buffer_load_dwordx4 v[172:175], v240, s[24:27], s71 offen\n\t"
      "buffer_load_dwordx2 v[216:217], v7, s[28:31], 0 offen\n\t"
      "buffer_load_dwordx2 v[218:219], v7, s[4:7], 0 offen\n\t"
      "buffer_load_dwordx2 v[220:221], v7, s[28:31], 0 offen offset:128\n\t"
      "buffer_load_dwordx2 v[222:223], v7, s[4:7], 0 offen offset:128\n\t"
      "s_waitcnt vmcnt(23)\n\t"
      "s_waitcnt lgkmcnt(0)\n\t"
      "s_barrier\n\t"
      "v_cvt_f32_i32 v32, v32\n\t"
      "v_cvt_f32_i32 v33, v33\n\t"
      "v_fma_f32 v32, v32, v16, v20\n\t"
      "v_fma_f32 v33, v33, v17, v21\n\t"
      "s_add_u32 m0, s14, 65536\n\t"
      "v_cvt_f32_i32 v36, v36\n\t"
      "v_cvt_f32_i32 v37, v37\n\t"
      "v_fma_f32 v36, v36, v16, v20\n\t"
      "v_fma_f32 v37, v37, v17, v21\n\t"
      "v_cvt_pk_bf16_f32 v192, v32, v36\n\t"
      "v_cvt_pk_bf16_f32 v196, v33, v37\n\t"
      "v_cvt_f32_i32 v40, v40\n\t"
      "s_cmp_lt_u32 s9, 2\n\t"
      "s_cselect_b32 s70, s46, s47\n\t"
      "s_mul_i32 s70, s70, 9216\n\t"
      "s_add_u32 s70, s70, s15\n\t"
      "buffer_load_dwordx4 v[96:99], v3, s[24:27], s70 offen\n\t"
      "v_cvt_f32_i32 v41, v41\n\t"
      "v_fma_f32 v40, v40, v16, v20\n\t"
      "v_fma_f32 v41, v41, v17, v21\n\t"
      "v_cvt_f32_i32 v44, v44\n\t"
      "v_cvt_f32_i32 v45, v45\n\t"
      "v_fma_f32 v44, v44, v16, v20\n\t"
      "v_fma_f32 v45, v45, v17, v21\n\t"
      "buffer_load_dwordx4 v1, s[20:23], s45 offen lds\n\t"
      "v_cvt_pk_bf16_f32 v193, v40, v44\n\t"
      "v_cvt_pk_bf16_f32 v197, v41, v45\n\t"
      "v_cvt_f32_i32 v48, v48\n\t"
      "v_cvt_f32_i32 v49, v49\n\t"
      "v_fma_f32 v48, v48, v16, v20\n\t"
      "v_fma_f32 v49, v49, v17, v21\n\t"
      "v_cvt_f32_i32 v52, v52\n\t"
      "buffer_load_dwordx4 v[100:103], v225, s[24:27], s70 offen\n\t"
      "v_cvt_f32_i32 v53, v53\n\t"
      "v_fma_f32 v52, v52, v16, v20\n\t"
      "v_fma_f32 v53, v53, v17, v21\n\t"
      "v_cvt_pk_bf16_f32 v194, v48, v52\n\t"
      "v_cvt_pk_bf16_f32 v198, v49, v53\n\t"
      "v_cvt_f32_i32 v56, v56\n\t"
      "v_cvt_f32_i32 v57, v57\n\t"
      "buffer_load_dwordx4 v233, s[20:23], s45 offen offset:1024 lds\n\t"
      "v_fma_f32 v56, v56, v16, v20\n\t"
      "v_fma_f32 v57, v57, v17, v21\n\t"
      "v_cvt_f32_i32 v60, v60\n\t"
      "v_cvt_f32_i32 v61, v61\n\t"
      "v_fma_f32 v60, v60, v16, v20\n\t"
      "v_fma_f32 v61, v61, v17, v21\n\t"
      "v_cvt_pk_bf16_f32 v195, v56, v60\n\t"
      "buffer_load_dwordx4 v[104:107], v226, s[24:27], s70 offen\n\t"
      "v_cvt_pk_bf16_f32 v199, v57, v61\n\t"
      "ds_write_b128 v4, v[192:195] offset:20480\n\t"
      "ds_write_b128 v4, v[196:199] offset:21504\n\t"
      "v_cvt_f32_i32 v34, v34\n\t"
      "v_cvt_f32_i32 v35, v35\n\t"
      "v_fma_f32 v34, v34, v18, v22\n\t"
      "v_fma_f32 v35, v35, v19, v23\n\t"
      "buffer_load_dwordx4 v234, s[20:23], s45 offen offset:2048 lds\n\t"
      "v_cvt_f32_i32 v38, v38\n\t"
      "v_cvt_f32_i32 v39, v39\n\t"
      "v_fma_f32 v38, v38, v18, v22\n\t"
      "v_fma_f32 v39, v39, v19, v23\n\t"
      "v_cvt_pk_bf16_f32 v192, v34, v38\n\t"
      "v_cvt_pk_bf16_f32 v196, v35, v39\n\t"
      "v_cvt_f32_i32 v42, v42\n\t"
      "buffer_load_dwordx4 v[108:111], v227, s[24:27], s70 offen\n\t"
      "v_cvt_f32_i32 v43, v43\n\t"
      "v_fma_f32 v42, v42, v18, v22\n\t"
      "v_fma_f32 v43, v43, v19, v23\n\t"
      "v_cvt_f32_i32 v46, v46\n\t"
      "v_cvt_f32_i32 v47, v47\n\t"
      "v_fma_f32 v46, v46, v18, v22\n\t"
      "v_fma_f32 v47, v47, v19, v23\n\t"
      "buffer_load_dwordx4 v235, s[20:23], s45 offen offset:3072 lds\n\t"
      "v_cvt_pk_bf16_f32 v193, v42, v46\n\t"
      "v_cvt_pk_bf16_f32 v197, v43, v47\n\t"
      "v_cvt_f32_i32 v50, v50\n\t"
      "v_cvt_f32_i32 v51, v51\n\t"
      "v_fma_f32 v50, v50, v18, v22\n\t"
      "v_fma_f32 v51, v51, v19, v23\n\t"
      "v_cvt_f32_i32 v54, v54\n\t"
      "buffer_load_dwordx4 v[112:115], v228, s[24:27], s70 offen\n\t"
      "v_cvt_f32_i32 v55, v55\n\t"
      "v_fma_f32 v54, v54, v18, v22\n\t"
      "v_fma_f32 v55, v55, v19, v23\n\t"
      "v_cvt_pk_bf16_f32 v194, v50, v54\n\t"
      "v_cvt_pk_bf16_f32 v198, v51, v55\n\t"
      "v_cvt_f32_i32 v58, v58\n\t"
      "v_cvt_f32_i32 v59, v59\n\t"
      "s_add_u32 m0, s14, 69632\n\t"
      "v_fma_f32 v58, v58, v18, v22\n\t"
      "v_fma_f32 v59, v59, v19, v23\n\t"
      "v_cvt_f32_i32 v62, v62\n\t"
      "v_cvt_f32_i32 v63, v63\n\t"
      "v_fma_f32 v62, v62, v18, v22\n\t"
      "v_fma_f32 v63, v63, v19, v23\n\t"
      "v_cvt_pk_bf16_f32 v195, v58, v62\n\t"
      "buffer_load_dwordx4 v[116:119], v229, s[24:27], s70 offen\n\t"
      "v_cvt_pk_bf16_f32 v199, v59, v63\n\t"
      "ds_write_b128 v4, v[192:195] offset:22528\n\t"
      "ds_write_b128 v4, v[196:199] offset:23552\n\t"
      "v_cvt_f32_i32 v160, v160\n\t"
      "v_cvt_f32_i32 v161, v161\n\t"
      "v_fma_f32 v160, v160, v24, v28\n\t"
      "v_fma_f32 v161, v161, v25, v29\n\t"
      "buffer_load_dwordx4 v2, s[20:23], s45 offen lds\n\t"
      "s_waitcnt vmcnt(33)\n\t"
      "v_cvt_f32_i32 v164, v164\n\t"
      "v_cvt_f32_i32 v165, v165\n\t"
      "v_fma_f32 v164, v164, v24, v28\n\t"
      "v_fma_f32 v165, v165, v25, v29\n\t"
      "v_cvt_pk_bf16_f32 v160, v160, v164\n\t"
      "ds_write_b32 v6, v160 offset:36864\n\t"
      "v_cvt_pk_bf16_f32 v161, v161, v165\n\t"
      "buffer_load_dwordx4 v[120:123], v230, s[24:27], s70 offen\n\t"
      "ds_write_b32 v6, v161 offset:37888\n\t"
      "v_cvt_f32_i32 v162, v162\n\t"
      "v_cvt_f32_i32 v163, v163\n\t"
      "v_fma_f32 v162, v162, v26, v30\n\t"
      "v_fma_f32 v163, v163, v27, v31\n\t"
      "v_cvt_f32_i32 v166, v166\n\t"
      "v_cvt_f32_i32 v167, v167\n\t"
      "buffer_load_dwordx4 v237, s[20:23], s45 offen offset:1024 lds\n\t"
      "v_fma_f32 v166, v166, v26, v30\n\t"
      "v_fma_f32 v167, v167, v27, v31\n\t"
      "v_cvt_pk_bf16_f32 v162, v162, v166\n\t"
      "ds_write_b32 v6, v162 offset:36992\n\t"
      "v_cvt_pk_bf16_f32 v163, v163, v167\n\t"
      "ds_write_b32 v6, v163 offset:38016\n\t"
      "buffer_load_dwordx4 v[124:127], v231, s[24:27], s70 offen\n\t"
      "buffer_load_dwordx4 v238, s[20:23], s45 offen offset:2048 lds\n\t"
      "buffer_load_dwordx4 v239, s[20:23], s45 offen offset:3072 lds\n\t"
      "s_waitcnt vmcnt(21)\n\t"
      "s_waitcnt lgkmcnt(0)\n\t"
      "s_barrier\n\t"
      "v_cvt_f32_i32 v64, v64\n\t"
      "v_cvt_f32_i32 v65, v65\n\t"
      "v_fma_f32 v64, v64, v16, v20\n\t"
      "v_fma_f32 v65, v65, v17, v21\n\t"
      "s_add_u32 m0, s14, 0\n\t"
      "v_cvt_f32_i32 v68, v68\n\t"
      "v_cvt_f32_i32 v69, v69\n\t"
      "v_fma_f32 v68, v68, v16, v20\n\t"
      "v_fma_f32 v69, v69, v17, v21\n\t"
      "v_cvt_pk_bf16_f32 v192, v64, v68\n\t"
      "v_cvt_pk_bf16_f32 v196, v65, v69\n\t"
      "v_cvt_f32_i32 v72, v72\n\t"
      "s_cmp_lt_u32 s9, 2\n\t"
      "s_cselect_b32 s70, s48, s46\n\t"
      "s_mul_i32 s70, s70, 9216\n\t"
      "s_add_u32 s70, s70, s16\n\t"
      "buffer_load_dwordx4 v[128:131], v3, s[24:27], s70 offen\n\t"
      "v_cvt_f32_i32 v73, v73\n\t"
      "v_fma_f32 v72, v72, v16, v20\n\t"
      "v_fma_f32 v73, v73, v17, v21\n\t"
      "v_cvt_f32_i32 v76, v76\n\t"
      "v_cvt_f32_i32 v77, v77\n\t"
      "v_fma_f32 v76, v76, v16, v20\n\t"
      "v_fma_f32 v77, v77, v17, v21\n\t"
      "buffer_load_dwordx4 v1, s[20:23], s46 offen lds\n\t"
      "v_cvt_pk_bf16_f32 v193, v72, v76\n\t"
      "v_cvt_pk_bf16_f32 v197, v73, v77\n\t"
      "v_cvt_f32_i32 v80, v80\n\t"
      "v_cvt_f32_i32 v81, v81\n\t"
      "v_fma_f32 v80, v80, v16, v20\n\t"
      "v_fma_f32 v81, v81, v17, v21\n\t"
      "v_cvt_f32_i32 v84, v84\n\t"
      "buffer_load_dwordx4 v[132:135], v225, s[24:27], s70 offen\n\t"
      "v_cvt_f32_i32 v85, v85\n\t"
      "v_fma_f32 v84, v84, v16, v20\n\t"
      "v_fma_f32 v85, v85, v17, v21\n\t"
      "v_cvt_pk_bf16_f32 v194, v80, v84\n\t"
      "v_cvt_pk_bf16_f32 v198, v81, v85\n\t"
      "v_cvt_f32_i32 v88, v88\n\t"
      "v_cvt_f32_i32 v89, v89\n\t"
      "buffer_load_dwordx4 v233, s[20:23], s46 offen offset:1024 lds\n\t"
      "v_fma_f32 v88, v88, v16, v20\n\t"
      "v_fma_f32 v89, v89, v17, v21\n\t"
      "v_cvt_f32_i32 v92, v92\n\t"
      "v_cvt_f32_i32 v93, v93\n\t"
      "v_fma_f32 v92, v92, v16, v20\n\t"
      "v_fma_f32 v93, v93, v17, v21\n\t"
      "v_cvt_pk_bf16_f32 v195, v88, v92\n\t"
      "v_cvt_pk_bf16_f32 v199, v89, v93\n\t"
      "buffer_load_dwordx4 v[136:139], v226, s[24:27], s70 offen\n\t"
      "ds_write_b128 v4, v[192:195] offset:40960\n\t"
      "ds_write_b128 v4, v[196:199] offset:41984\n\t"
      "v_cvt_f32_i32 v66, v66\n\t"
      "v_cvt_f32_i32 v67, v67\n\t"
      "v_fma_f32 v66, v66, v18, v22\n\t"
      "v_fma_f32 v67, v67, v19, v23\n\t"
      "v_cvt_f32_i32 v70, v70\n\t"
      "buffer_load_dwordx4 v234, s[20:23], s46 offen offset:2048 lds\n\t"
      "v_cvt_f32_i32 v71, v71\n\t"
      "v_fma_f32 v70, v70, v18, v22\n\t"
      "v_fma_f32 v71, v71, v19, v23\n\t"
      "v_cvt_pk_bf16_f32 v192, v66, v70\n\t"
      "v_cvt_pk_bf16_f32 v196, v67, v71\n\t"
      "v_cvt_f32_i32 v74, v74\n\t"
      "v_cvt_f32_i32 v75, v75\n\t"
      "buffer_load_dwordx4 v[140:143], v227, s[24:27], s70 offen\n\t"
      "v_fma_f32 v74, v74, v18, v22\n\t"
      "v_fma_f32 v75, v75, v19, v23\n\t"
      "v_cvt_f32_i32 v78, v78\n\t"
      "v_cvt_f32_i32 v79, v79\n\t"
      "v_fma_f32 v78, v78, v18, v22\n\t"
      "v_fma_f32 v79, v79, v19, v23\n\t"
      "v_cvt_pk_bf16_f32 v193, v74, v78\n\t"
      "buffer_load_dwordx4 v235, s[20:23], s46 offen offset:3072 lds\n\t"
      "v_cvt_pk_bf16_f32 v197, v75, v79\n\t"
      "v_cvt_f32_i32 v82, v82\n\t"
      "v_cvt_f32_i32 v83, v83\n\t"
      "v_fma_f32 v82, v82, v18, v22\n\t"
      "v_fma_f32 v83, v83, v19, v23\n\t"
      "v_cvt_f32_i32 v86, v86\n\t"
      "v_cvt_f32_i32 v87, v87\n\t"
      "buffer_load_dwordx4 v[144:147], v228, s[24:27], s70 offen\n\t"
      "v_fma_f32 v86, v86, v18, v22\n\t"
      "v_fma_f32 v87, v87, v19, v23\n\t"
      "v_cvt_pk_bf16_f32 v194, v82, v86\n\t"
      "v_cvt_pk_bf16_f32 v198, v83, v87\n\t"
      "v_cvt_f32_i32 v90, v90\n\t"
      "v_cvt_f32_i32 v91, v91\n\t"
      "v_fma_f32 v90, v90, v18, v22\n\t"
      "s_add_u32 m0, s14, 4096\n\t"
      "v_fma_f32 v91, v91, v19, v23\n\t"
      "v_cvt_f32_i32 v94, v94\n\t"
      "v_cvt_f32_i32 v95, v95\n\t"
      "v_fma_f32 v94, v94, v18, v22\n\t"
      "v_fma_f32 v95, v95, v19, v23\n\t"
      "v_cvt_pk_bf16_f32 v195, v90, v94\n\t"
      "v_cvt_pk_bf16_f32 v199, v91, v95\n\t"
      "ds_write_b128 v4, v[192:195] offset:43008\n\t"
      "buffer_load_dwordx4 v[148:151], v229, s[24:27], s70 offen\n\t"
      "ds_write_b128 v4, v[196:199] offset:44032\n\t"
      "v_cvt_f32_i32 v168, v168\n\t"
      "v_cvt_f32_i32 v169, v169\n\t"
      "v_fma_f32 v168, v168, v24, v28\n\t"
      "v_fma_f32 v169, v169, v25, v29\n\t"
      "s_waitcnt vmcnt(30)\n\t"
      "v_cvt_f32_i32 v172, v172\n\t"
      "v_cvt_f32_i32 v173, v173\n\t"
      "buffer_load_dwordx4 v2, s[20:23], s46 offen lds\n\t"
      "v_fma_f32 v172, v172, v24, v28\n\t"
      "v_fma_f32 v173, v173, v25, v29\n\t"
      "v_cvt_pk_bf16_f32 v168, v168, v172\n\t"
      "ds_write_b32 v6, v168 offset:57344\n\t"
      "v_cvt_pk_bf16_f32 v169, v169, v173\n\t"
      "ds_write_b32 v6, v169 offset:58368\n\t"
      "v_cvt_f32_i32 v170, v170\n\t"
      "buffer_load_dwordx4 v[152:155], v230, s[24:27], s70 offen\n\t"
      "v_cvt_f32_i32 v171, v171\n\t"
      "v_fma_f32 v170, v170, v26, v30\n\t"
      "v_fma_f32 v171, v171, v27, v31\n\t"
      "v_cvt_f32_i32 v174, v174\n\t"
      "v_cvt_f32_i32 v175, v175\n\t"
      "v_fma_f32 v174, v174, v26, v30\n\t"
      "v_fma_f32 v175, v175, v27, v31\n\t"
      "buffer_load_dwordx4 v237, s[20:23], s46 offen offset:1024 lds\n\t"
      "v_cvt_pk_bf16_f32 v170, v170, v174\n\t"
      "ds_write_b32 v6, v170 offset:57472\n\t"
      "v_cvt_pk_bf16_f32 v171, v171, v175\n\t"
      "ds_write_b32 v6, v171 offset:58496\n\t"
      "s_waitcnt vmcnt(32)\n\t"
      "v_lshlrev_b32 v200, 16, v216\n\t"
      "s_waitcnt vmcnt(31)\n\t"
      "v_lshlrev_b32 v204, 16, v218\n\t"
      "v_mul_f32 v204, -v204, v200\n\t"
      "v_and_b32 v201, 0xffff0000, v216\n\t"
      "v_and_b32 v205, 0xffff0000, v218\n\t"
      "v_mul_f32 v205, -v205, v201\n\t"
      "v_lshlrev_b32 v202, 16, v217\n\t"
      "v_lshlrev_b32 v206, 16, v219\n\t"
      "v_mul_f32 v206, -v206, v202\n\t"
      "v_and_b32 v203, 0xffff0000, v217\n\t"
      "v_and_b32 v207, 0xffff0000, v219\n\t"
      "v_mul_f32 v207, -v207, v203\n\t"
      "s_waitcnt vmcnt(30)\n\t"
      "v_lshlrev_b32 v208, 16, v220\n\t"
      "s_waitcnt vmcnt(29)\n\t"
      "v_lshlrev_b32 v212, 16, v222\n\t"
      "v_mul_f32 v212, -v212, v208\n\t"
      "v_and_b32 v209, 0xffff0000, v220\n\t"
      "v_and_b32 v213, 0xffff0000, v222\n\t"
      "v_mul_f32 v213, -v213, v209\n\t"
      "v_lshlrev_b32 v210, 16, v221\n\t"
      "v_lshlrev_b32 v214, 16, v223\n\t"
      "v_mul_f32 v214, -v214, v210\n\t"
      "v_and_b32 v211, 0xffff0000, v221\n\t"
      "v_and_b32 v215, 0xffff0000, v223\n\t"
      "v_mul_f32 v215, -v215, v211\n\t"
      "buffer_load_dwordx4 v[156:159], v231, s[24:27], s70 offen\n\t"
      "buffer_load_dwordx4 v238, s[20:23], s46 offen offset:2048 lds\n\t"
      "buffer_load_dwordx4 v239, s[20:23], s46 offen offset:3072 lds\n\t"
      "s_waitcnt vmcnt(16)\n\t"
      "s_waitcnt lgkmcnt(0)\n\t"
      "s_barrier\n\t"
      "s_add_u32 m0, s14, 32768\n\t"
      "s_nop 0\n\t"
      "buffer_load_dwordx4 v1, s[20:23], s47 offen lds\n\t"
      "buffer_load_dwordx4 v233, s[20:23], s47 offen offset:1024 lds\n\t"
      "buffer_load_dwordx4 v234, s[20:23], s47 offen offset:2048 lds\n\t"
      "buffer_load_dwordx4 v235, s[20:23], s47 offen offset:3072 lds\n\t"
      "s_add_u32 m0, s14, 36864\n\t"
      "s_nop 0\n\t"
      "buffer_load_dwordx4 v2, s[20:23], s47 offen lds\n\t"
      "buffer_load_dwordx4 v237, s[20:23], s47 offen offset:1024 lds\n\t"
      "buffer_load_dwordx4 v238, s[20:23], s47 offen offset:2048 lds\n\t"
      "buffer_load_dwordx4 v239, s[20:23], s47 offen offset:3072 lds\n\t"
      "s_cmp_lt_u32 s9, 2\n\t"
      "s_mov_b32 s50, 0\n\t"
      "s_cselect_b32 s50, s50, 16384\n\t"
      "v_add_u32 v9, s50, v4\n\t"
      "v_cvt_f32_i32 v96, v96\n\t"
      "v_cvt_f32_i32 v97, v97\n\t"
      "v_fma_f32 v96, v96, v200, v204\n\t"
      "v_fma_f32 v97, v97, v201, v205\n\t"
      "v_cvt_f32_i32 v100, v100\n\t"
      "v_cvt_f32_i32 v101, v101\n\t"
      "v_fma_f32 v100, v100, v200, v204\n\t"
      "v_fma_f32 v101, v101, v201, v205\n\t"
      "v_cvt_pk_bf16_f32 v192, v96, v100\n\t"
      "v_cvt_pk_bf16_f32 v196, v97, v101\n\t"
      "v_cvt_f32_i32 v104, v104\n\t"
      "v_cvt_f32_i32 v105, v105\n\t"
      "v_fma_f32 v104, v104, v200, v204\n\t"
      "v_fma_f32 v105, v105, v201, v205\n\t"
      "v_cvt_f32_i32 v108, v108\n\t"
      "v_cvt_f32_i32 v109, v109\n\t"
      "v_fma_f32 v108, v108, v200, v204\n\t"
      "v_fma_f32 v109, v109, v201, v205\n\t"
      "v_cvt_pk_bf16_f32 v193, v104, v108\n\t"
      "v_cvt_pk_bf16_f32 v197, v105, v109\n\t"
      "v_cvt_f32_i32 v112, v112\n\t"
      "v_cvt_f32_i32 v113, v113\n\t"
      "v_fma_f32 v112, v112, v200, v204\n\t"
      "v_fma_f32 v113, v113, v201, v205\n\t"
      "v_cvt_f32_i32 v116, v116\n\t"
      "v_cvt_f32_i32 v117, v117\n\t"
      "v_fma_f32 v116, v116, v200, v204\n\t"
      "v_fma_f32 v117, v117, v201, v205\n\t"
      "v_cvt_pk_bf16_f32 v194, v112, v116\n\t"
      "v_cvt_pk_bf16_f32 v198, v113, v117\n\t"
      "v_cvt_f32_i32 v120, v120\n\t"
      "v_cvt_f32_i32 v121, v121\n\t"
      "v_fma_f32 v120, v120, v200, v204\n\t"
      "v_fma_f32 v121, v121, v201, v205\n\t"
      "v_cvt_f32_i32 v124, v124\n\t"
      "v_cvt_f32_i32 v125, v125\n\t"
      "v_fma_f32 v124, v124, v200, v204\n\t"
      "v_fma_f32 v125, v125, v201, v205\n\t"
      "v_cvt_pk_bf16_f32 v195, v120, v124\n\t"
      "v_cvt_pk_bf16_f32 v199, v121, v125\n\t"
      "ds_write_b128 v9, v[192:195] offset:0\n\t"
      "ds_write_b128 v9, v[196:199] offset:1024\n\t"
      "v_cvt_f32_i32 v98, v98\n\t"
      "v_cvt_f32_i32 v99, v99\n\t"
      "v_fma_f32 v98, v98, v202, v206\n\t"
      "v_fma_f32 v99, v99, v203, v207\n\t"
      "v_cvt_f32_i32 v102, v102\n\t"
      "v_cvt_f32_i32 v103, v103\n\t"
      "v_fma_f32 v102, v102, v202, v206\n\t"
      "v_fma_f32 v103, v103, v203, v207\n\t"
      "v_cvt_pk_bf16_f32 v192, v98, v102\n\t"
      "v_cvt_pk_bf16_f32 v196, v99, v103\n\t"
      "v_cvt_f32_i32 v106, v106\n\t"
      "v_cvt_f32_i32 v107, v107\n\t"
      "v_fma_f32 v106, v106, v202, v206\n\t"
      "v_fma_f32 v107, v107, v203, v207\n\t"
      "v_cvt_f32_i32 v110, v110\n\t"
      "v_cvt_f32_i32 v111, v111\n\t"
      "v_fma_f32 v110, v110, v202, v206\n\t"
      "v_fma_f32 v111, v111, v203, v207\n\t"
      "v_cvt_pk_bf16_f32 v193, v106, v110\n\t"
      "v_cvt_pk_bf16_f32 v197, v107, v111\n\t"
      "v_cvt_f32_i32 v114, v114\n\t"
      "v_cvt_f32_i32 v115, v115\n\t"
      "v_fma_f32 v114, v114, v202, v206\n\t"
      "v_fma_f32 v115, v115, v203, v207\n\t"
      "v_cvt_f32_i32 v118, v118\n\t"
      "v_cvt_f32_i32 v119, v119\n\t"
      "v_fma_f32 v118, v118, v202, v206\n\t"
      "v_fma_f32 v119, v119, v203, v207\n\t"
      "v_cvt_pk_bf16_f32 v194, v114, v118\n\t"
      "v_cvt_pk_bf16_f32 v198, v115, v119\n\t"
      "v_cvt_f32_i32 v122, v122\n\t"
      "v_cvt_f32_i32 v123, v123\n\t"
      "v_fma_f32 v122, v122, v202, v206\n\t"
      "v_fma_f32 v123, v123, v203, v207\n\t"
      "v_cvt_f32_i32 v126, v126\n\t"
      "v_cvt_f32_i32 v127, v127\n\t"
      "v_fma_f32 v126, v126, v202, v206\n\t"
      "v_fma_f32 v127, v127, v203, v207\n\t"
      "v_cvt_pk_bf16_f32 v195, v122, v126\n\t"
      "v_cvt_pk_bf16_f32 v199, v123, v127\n\t"
      "ds_write_b128 v9, v[192:195] offset:2048\n\t"
      "ds_write_b128 v9, v[196:199] offset:3072\n\t"
      "s_waitcnt vmcnt(0)\n\t"
      "s_waitcnt lgkmcnt(0)\n\t"
      "s_barrier\n\t"
      "s_add_u32 m0, s14, 65536\n\t"
      "s_nop 0\n\t"
      "buffer_load_dwordx4 v1, s[20:23], s48 offen lds\n\t"
      "buffer_load_dwordx4 v233, s[20:23], s48 offen offset:1024 lds\n\t"
      "buffer_load_dwordx4 v234, s[20:23], s48 offen offset:2048 lds\n\t"
      "buffer_load_dwordx4 v235, s[20:23], s48 offen offset:3072 lds\n\t"
      "s_add_u32 m0, s14, 69632\n\t"
      "s_nop 0\n\t"
      "buffer_load_dwordx4 v2, s[20:23], s48 offen lds\n\t"
      "buffer_load_dwordx4 v237, s[20:23], s48 offen offset:1024 lds\n\t"
      "buffer_load_dwordx4 v238, s[20:23], s48 offen offset:2048 lds\n\t"
      "buffer_load_dwordx4 v239, s[20:23], s48 offen offset:3072 lds\n\t"
      "s_cmp_lt_u32 s9, 2\n\t"
      "s_mov_b32 s50, 40960\n\t"
      "s_cselect_b32 s50, s50, 0\n\t"
      "v_add_u32 v9, s50, v4\n\t"
      "v_cvt_f32_i32 v128, v128\n\t"
      "v_cvt_f32_i32 v129, v129\n\t"
      "v_fma_f32 v128, v128, v16, v20\n\t"
      "v_fma_f32 v129, v129, v17, v21\n\t"
      "v_cvt_f32_i32 v132, v132\n\t"
      "s_cmp_lt_u32 s9, 2\n\t"
      "s_cselect_b32 s70, s47, s48\n\t"
      "s_mul_i32 s70, s70, 9216\n\t"
      "s_add_u32 s70, s70, s15\n\t"
      "buffer_load_dwordx4 v[32:35], v3, s[24:27], s70 offen offset:256\n\t"
      "v_cvt_f32_i32 v133, v133\n\t"
      "v_fma_f32 v132, v132, v16, v20\n\t"
      "v_fma_f32 v133, v133, v17, v21\n\t"
      "v_cvt_pk_bf16_f32 v192, v128, v132\n\t"
      "v_cvt_pk_bf16_f32 v196, v129, v133\n\t"
      "v_cvt_f32_i32 v136, v136\n\t"
      "v_cvt_f32_i32 v137, v137\n\t"
      "v_fma_f32 v136, v136, v16, v20\n\t"
      "v_fma_f32 v137, v137, v17, v21\n\t"
      "v_cvt_f32_i32 v140, v140\n\t"
      "v_cvt_f32_i32 v141, v141\n\t"
      "v_fma_f32 v140, v140, v16, v20\n\t"
      "v_fma_f32 v141, v141, v17, v21\n\t"
      "buffer_load_dwordx4 v[36:39], v225, s[24:27], s70 offen offset:256\n\t"
      "v_cvt_pk_bf16_f32 v193, v136, v140\n\t"
      "v_cvt_pk_bf16_f32 v197, v137, v141\n\t"
      "v_cvt_f32_i32 v144, v144\n\t"
      "v_cvt_f32_i32 v145, v145\n\t"
      "v_fma_f32 v144, v144, v16, v20\n\t"
      "v_fma_f32 v145, v145, v17, v21\n\t"
      "v_cvt_f32_i32 v148, v148\n\t"
      "v_cvt_f32_i32 v149, v149\n\t"
      "v_fma_f32 v148, v148, v16, v20\n\t"
      "v_fma_f32 v149, v149, v17, v21\n\t"
      "v_cvt_pk_bf16_f32 v194, v144, v148\n\t"
      "v_cvt_pk_bf16_f32 v198, v145, v149\n\t"
      "buffer_load_dwordx4 v[40:43], v226, s[24:27], s70 offen offset:256\n\t"
      "v_cvt_f32_i32 v152, v152\n\t"
      "v_cvt_f32_i32 v153, v153\n\t"
      "v_fma_f32 v152, v152, v16, v20\n\t"
      "v_fma_f32 v153, v153, v17, v21\n\t"
      "v_cvt_f32_i32 v156, v156\n\t"
      "v_cvt_f32_i32 v157, v157\n\t"
      "v_fma_f32 v156, v156, v16, v20\n\t"
      "v_fma_f32 v157, v157, v17, v21\n\t"
      "v_cvt_pk_bf16_f32 v195, v152, v156\n\t"
      "v_cvt_pk_bf16_f32 v199, v153, v157\n\t"
      "ds_write_b128 v9, v[192:195] offset:0\n\t"
      "ds_write_b128 v9, v[196:199] offset:1024\n\t"
      "v_cvt_f32_i32 v130, v130\n\t"
      "buffer_load_dwordx4 v[44:47], v227, s[24:27], s70 offen offset:256\n\t"
      "v_cvt_f32_i32 v131, v131\n\t"
      "v_fma_f32 v130, v130, v18, v22\n\t"
      "v_fma_f32 v131, v131, v19, v23\n\t"
      "v_cvt_f32_i32 v134, v134\n\t"
      "v_cvt_f32_i32 v135, v135\n\t"
      "v_fma_f32 v134, v134, v18, v22\n\t"
      "v_fma_f32 v135, v135, v19, v23\n\t"
      "v_cvt_pk_bf16_f32 v192, v130, v134\n\t"
      "v_cvt_pk_bf16_f32 v196, v131, v135\n\t"
      "v_cvt_f32_i32 v138, v138\n\t"
      "v_cvt_f32_i32 v139, v139\n\t"
      "v_fma_f32 v138, v138, v18, v22\n\t"
      "buffer_load_dwordx4 v[48:51], v228, s[24:27], s70 offen offset:256\n\t"
      "v_fma_f32 v139, v139, v19, v23\n\t"
      "v_cvt_f32_i32 v142, v142\n\t"
      "v_cvt_f32_i32 v143, v143\n\t"
      "v_fma_f32 v142, v142, v18, v22\n\t"
      "v_fma_f32 v143, v143, v19, v23\n\t"
      "v_cvt_pk_bf16_f32 v193, v138, v142\n\t"
      "v_cvt_pk_bf16_f32 v197, v139, v143\n\t"
      "v_cvt_f32_i32 v146, v146\n\t"
      "v_cvt_f32_i32 v147, v147\n\t"
      "v_fma_f32 v146, v146, v18, v22\n\t"
      "v_fma_f32 v147, v147, v19, v23\n\t"
      "v_cvt_f32_i32 v150, v150\n\t"
      "v_cvt_f32_i32 v151, v151\n\t"
      "buffer_load_dwordx4 v[52:55], v229, s[24:27], s70 offen offset:256\n\t"
      "v_fma_f32 v150, v150, v18, v22\n\t"
      "v_fma_f32 v151, v151, v19, v23\n\t"
      "v_cvt_pk_bf16_f32 v194, v146, v150\n\t"
      "v_cvt_pk_bf16_f32 v198, v147, v151\n\t"
      "v_cvt_f32_i32 v154, v154\n\t"
      "v_cvt_f32_i32 v155, v155\n\t"
      "v_fma_f32 v154, v154, v18, v22\n\t"
      "v_fma_f32 v155, v155, v19, v23\n\t"
      "v_cvt_f32_i32 v158, v158\n\t"
      "v_cvt_f32_i32 v159, v159\n\t"
      "v_fma_f32 v158, v158, v18, v22\n\t"
      "v_fma_f32 v159, v159, v19, v23\n\t"
      "buffer_load_dwordx4 v[56:59], v230, s[24:27], s70 offen offset:256\n\t"
      "v_cvt_pk_bf16_f32 v195, v154, v158\n\t"
      "v_cvt_pk_bf16_f32 v199, v155, v159\n\t"
      "ds_write_b128 v9, v[192:195] offset:2048\n\t"
      "ds_write_b128 v9, v[196:199] offset:3072\n\t"
      "buffer_load_dwordx4 v[60:63], v231, s[24:27], s70 offen offset:256\n\t"
      "s_waitcnt vmcnt(8)\n\t"
      "s_waitcnt lgkmcnt(0)\n\t"
      "s_barrier\n\t"
      "s_mul_i32 s71, s46, 9216\n\t"
      "buffer_load_dwordx4 v[176:179], v5, s[24:27], s71 offen\n\t"
      "buffer_load_dwordx4 v[180:183], v240, s[24:27], s71 offen\n\t"
      "s_mul_i32 s71, s47, 9216\n\t"
      "buffer_load_dwordx4 v[184:187], v5, s[24:27], s71 offen\n\t"
      "buffer_load_dwordx4 v[188:191], v240, s[24:27], s71 offen\n\t"
      "s_mul_i32 s71, s48, 9216\n\t"
      "buffer_load_dwordx4 v[160:163], v5, s[24:27], s71 offen\n\t"
      "buffer_load_dwordx4 v[164:167], v240, s[24:27], s71 offen\n\t"
      "s_cmp_lt_u32 s9, 2\n\t"
      "s_mov_b32 s50, 24576\n\t"
      "s_cselect_b32 s50, s50, 40960\n\t"
      "v_add_u32 v9, s50, v4\n\t"
      "s_waitcnt vmcnt(13)\n\t"
      "v_cvt_f32_i32 v32, v32\n\t"
      "v_cvt_f32_i32 v33, v33\n\t"
      "v_fma_f32 v32, v32, v208, v212\n\t"
      "v_fma_f32 v33, v33, v209, v213\n\t"
      "s_waitcnt vmcnt(12)\n\t"
      "v_cvt_f32_i32 v36, v36\n\t"
      "v_cvt_f32_i32 v37, v37\n\t"
      "v_fma_f32 v36, v36, v208, v212\n\t"
      "v_fma_f32 v37, v37, v209, v213\n\t"
      "v_cvt_pk_bf16_f32 v192, v32, v36\n\t"
      "v_cvt_pk_bf16_f32 v196, v33, v37\n\t"
      "s_waitcnt vmcnt(11)\n\t"
      "v_cvt_f32_i32 v40, v40\n\t"
      "v_cvt_f32_i32 v41, v41\n\t"
      "v_fma_f32 v40, v40, v208, v212\n\t"
      "v_fma_f32 v41, v41, v209, v213\n\t"
      "s_waitcnt vmcnt(10)\n\t"
      "v_cvt_f32_i32 v44, v44\n\t"
      "v_cvt_f32_i32 v45, v45\n\t"
      "v_fma_f32 v44, v44, v208, v212\n\t"
      "v_fma_f32 v45, v45, v209, v213\n\t"
      "v_cvt_pk_bf16_f32 v193, v40, v44\n\t"
      "v_cvt_pk_bf16_f32 v197, v41, v45\n\t"
      "s_waitcnt vmcnt(9)\n\t"
      "v_cvt_f32_i32 v48, v48\n\t"
      "v_cvt_f32_i32 v49, v49\n\t"
      "v_fma_f32 v48, v48, v208, v212\n\t"
      "v_fma_f32 v49, v49, v209, v213\n\t"
      "s_waitcnt vmcnt(8)\n\t"
      "v_cvt_f32_i32 v52, v52\n\t"
      "v_cvt_f32_i32 v53, v53\n\t"
      "v_fma_f32 v52, v52, v208, v212\n\t"
      "v_fma_f32 v53, v53, v209, v213\n\t"
      "v_cvt_pk_bf16_f32 v194, v48, v52\n\t"
      "v_cvt_pk_bf16_f32 v198, v49, v53\n\t"
      "s_waitcnt vmcnt(7)\n\t"
      "v_cvt_f32_i32 v56, v56\n\t"
      "v_cvt_f32_i32 v57, v57\n\t"
      "v_fma_f32 v56, v56, v208, v212\n\t"
      "v_fma_f32 v57, v57, v209, v213\n\t"
      "s_waitcnt vmcnt(6)\n\t"
      "v_cvt_f32_i32 v60, v60\n\t"
      "v_cvt_f32_i32 v61, v61\n\t"
      "v_fma_f32 v60, v60, v208, v212\n\t"
      "v_fma_f32 v61, v61, v209, v213\n\t"
      "v_cvt_pk_bf16_f32 v195, v56, v60\n\t"
      "v_cvt_pk_bf16_f32 v199, v57, v61\n\t"
      "ds_write_b128 v9, v[192:195] offset:0\n\t"
      "ds_write_b128 v9, v[196:199] offset:1024\n\t"
      "v_cvt_f32_i32 v34, v34\n\t"
      "v_cvt_f32_i32 v35, v35\n\t"
      "v_fma_f32 v34, v34, v210, v214\n\t"
      "v_fma_f32 v35, v35, v211, v215\n\t"
      "v_cvt_f32_i32 v38, v38\n\t"
      "v_cvt_f32_i32 v39, v39\n\t"
      "v_fma_f32 v38, v38, v210, v214\n\t"
      "v_fma_f32 v39, v39, v211, v215\n\t"
      "v_cvt_pk_bf16_f32 v192, v34, v38\n\t"
      "v_cvt_pk_bf16_f32 v196, v35, v39\n\t"
      "v_cvt_f32_i32 v42, v42\n\t"
      "v_cvt_f32_i32 v43, v43\n\t"
      "v_fma_f32 v42, v42, v210, v214\n\t"
      "v_fma_f32 v43, v43, v211, v215\n\t"
      "v_cvt_f32_i32 v46, v46\n\t"
      "v_cvt_f32_i32 v47, v47\n\t"
      "v_fma_f32 v46, v46, v210, v214\n\t"
      "v_fma_f32 v47, v47, v211, v215\n\t"
      "v_cvt_pk_bf16_f32 v193, v42, v46\n\t"
      "v_cvt_pk_bf16_f32 v197, v43, v47\n\t"
      "v_cvt_f32_i32 v50, v50\n\t"
      "v_cvt_f32_i32 v51, v51\n\t"
      "v_fma_f32 v50, v50, v210, v214\n\t"
      "v_fma_f32 v51, v51, v211, v215\n\t"
      "v_cvt_f32_i32 v54, v54\n\t"
      "v_cvt_f32_i32 v55, v55\n\t"
      "v_fma_f32 v54, v54, v210, v214\n\t"
      "v_fma_f32 v55, v55, v211, v215\n\t"
      "v_cvt_pk_bf16_f32 v194, v50, v54\n\t"
      "v_cvt_pk_bf16_f32 v198, v51, v55\n\t"
      "v_cvt_f32_i32 v58, v58\n\t"
      "v_cvt_f32_i32 v59, v59\n\t"
      "v_fma_f32 v58, v58, v210, v214\n\t"
      "v_fma_f32 v59, v59, v211, v215\n\t"
      "v_cvt_f32_i32 v62, v62\n\t"
      "v_cvt_f32_i32 v63, v63\n\t"
      "v_fma_f32 v62, v62, v210, v214\n\t"
      "v_fma_f32 v63, v63, v211, v215\n\t"
      "v_cvt_pk_bf16_f32 v195, v58, v62\n\t"
      "v_cvt_pk_bf16_f32 v199, v59, v63\n\t"
      "ds_write_b128 v9, v[192:195] offset:2048\n\t"
      "ds_write_b128 v9, v[196:199] offset:3072\n\t"
      "s_waitcnt lgkmcnt(0)\n\t"
      "s_barrier\n\t"
      "s_waitcnt vmcnt(5)\n\t"
      "v_cvt_f32_i32 v176, v176\n\t"
      "v_cvt_f32_i32 v177, v177\n\t"
      "v_fma_f32 v176, v176, v24, v28\n\t"
      "v_fma_f32 v177, v177, v25, v29\n\t"
      "s_waitcnt vmcnt(4)\n\t"
      "v_cvt_f32_i32 v180, v180\n\t"
      "v_cvt_f32_i32 v181, v181\n\t"
      "v_fma_f32 v180, v180, v24, v28\n\t"
      "v_fma_f32 v181, v181, v25, v29\n\t"
      "v_cvt_pk_bf16_f32 v176, v176, v180\n\t"
      "ds_write_b32 v6, v176 offset:16384\n\t"
      "v_cvt_pk_bf16_f32 v177, v177, v181\n\t"
      "ds_write_b32 v6, v177 offset:17408\n\t"
      "v_cvt_f32_i32 v178, v178\n\t"
      "v_cvt_f32_i32 v179, v179\n\t"
      "v_fma_f32 v178, v178, v26, v30\n\t"
      "v_fma_f32 v179, v179, v27, v31\n\t"
      "v_cvt_f32_i32 v182, v182\n\t"
      "v_cvt_f32_i32 v183, v183\n\t"
      "v_fma_f32 v182, v182, v26, v30\n\t"
      "v_fma_f32 v183, v183, v27, v31\n\t"
      "v_cvt_pk_bf16_f32 v178, v178, v182\n\t"
      "ds_write_b32 v6, v178 offset:16512\n\t"
      "v_cvt_pk_bf16_f32 v179, v179, v183\n\t"
      "ds_write_b32 v6, v179 offset:17536\n\t"
      "s_waitcnt vmcnt(3)\n\t"
      "v_cvt_f32_i32 v184, v184\n\t"
      "v_cvt_f32_i32 v185, v185\n\t"
      "v_fma_f32 v184, v184, v24, v28\n\t"
      "v_fma_f32 v185, v185, v25, v29\n\t"
      "s_waitcnt vmcnt(2)\n\t"
      "v_cvt_f32_i32 v188, v188\n\t"
      "v_cvt_f32_i32 v189, v189\n\t"
      "v_fma_f32 v188, v188, v24, v28\n\t"
      "v_fma_f32 v189, v189, v25, v29\n\t"
      "v_cvt_pk_bf16_f32 v184, v184, v188\n\t"
      "ds_write_b32 v6, v184 offset:36864\n\t"
      "v_cvt_pk_bf16_f32 v185, v185, v189\n\t"
      "ds_write_b32 v6, v185 offset:37888\n\t"
      "v_cvt_f32_i32 v186, v186\n\t"
      "v_cvt_f32_i32 v187, v187\n\t"
      "v_fma_f32 v186, v186, v26, v30\n\t"
      "v_fma_f32 v187, v187, v27, v31\n\t"
      "v_cvt_f32_i32 v190, v190\n\t"
      "v_cvt_f32_i32 v191, v191\n\t"
      "v_fma_f32 v190, v190, v26, v30\n\t"
      "v_fma_f32 v191, v191, v27, v31\n\t"
      "v_cvt_pk_bf16_f32 v186, v186, v190\n\t"
      "ds_write_b32 v6, v186 offset:36992\n\t"
      "v_cvt_pk_bf16_f32 v187, v187, v191\n\t"
      "ds_write_b32 v6, v187 offset:38016\n\t"
      "s_waitcnt vmcnt(1)\n\t"
      "v_cvt_f32_i32 v160, v160\n\t"
      "v_cvt_f32_i32 v161, v161\n\t"
      "v_fma_f32 v160, v160, v24, v28\n\t"
      "v_fma_f32 v161, v161, v25, v29\n\t"
      "s_waitcnt vmcnt(0)\n\t"
      "v_cvt_f32_i32 v164, v164\n\t"
      "v_cvt_f32_i32 v165, v165\n\t"
      "v_fma_f32 v164, v164, v24, v28\n\t"
      "v_fma_f32 v165, v165, v25, v29\n\t"
      "v_cvt_pk_bf16_f32 v160, v160, v164\n\t"
      "ds_write_b32 v6, v160 offset:57344\n\t"
      "v_cvt_pk_bf16_f32 v161, v161, v165\n\t"
      "ds_write_b32 v6, v161 offset:58368\n\t"
      "v_cvt_f32_i32 v162, v162\n\t"
      "v_cvt_f32_i32 v163, v163\n\t"
      "v_fma_f32 v162, v162, v26, v30\n\t"
      "v_fma_f32 v163, v163, v27, v31\n\t"
      "v_cvt_f32_i32 v166, v166\n\t"
      "v_cvt_f32_i32 v167, v167\n\t"
      "v_fma_f32 v166, v166, v26, v30\n\t"
      "v_fma_f32 v167, v167, v27, v31\n\t"
      "v_cvt_pk_bf16_f32 v162, v162, v166\n\t"
      "ds_write_b32 v6, v162 offset:57472\n\t"
      "v_cvt_pk_bf16_f32 v163, v163, v167\n\t"
      "ds_write_b32 v6, v163 offset:58496\n\t"
      "s_waitcnt lgkmcnt(0)\n\t"
      "s_barrier\n\t"
      "s_branch LE_%=\n\t"
      "LC_%=:\n\t"
      "s_setprio 3\n\t"
      "s_lshl_b32 s17, s10, 8\n\t"
      "s_lshl_b32 s50, s9, 5\n\t"
      "s_add_u32 s17, s17, s50\n\t"
      "s_and_b32 s50, %[bid], 0x80\n\t"
      "s_xor_b32 s51, s50, 0x80\n\t"
      "s_add_u32 s18, s17, s51\n\t"
      "s_add_u32 s17, s17, s50\n\t"
      "v_bfe_u32 v11, v0, 3, 3\n\t"
      "v_xor_b32 v12, v11, v0\n\t"
      "v_and_b32 v12, 7, v12\n\t"
      "v_mul_u32_u24 v11, 1152, v11\n\t"
      "v_lshl_add_u32 v11, v12, 4, v11\n\t"
      "s_mul_i32 s50, s17, 1152\n\t"
      "s_mul_i32 s51, s18, 1152\n\t"
      "v_add_u32 v1, s50, v11\n\t"
      "v_add_u32 v2, s51, v11\n\t"
      "s_lshl_b32 s14, s9, 13\n\t"
      "s_add_u32 s14, s14, %[lds]\n\t"
      "s_add_u32 s14, s14, 61440\n\t"
      "v_add_u32 v233, 8192, v1\n\t"
      "v_add_u32 v234, 16384, v1\n\t"
      "v_add_u32 v235, 24576, v1\n\t"
      "v_add_u32 v237, 8192, v2\n\t"
      "v_add_u32 v238, 16384, v2\n\t"
      "v_add_u32 v239, 24576, v2\n\t"
      "s_waitcnt lgkmcnt(0)\n\t"
      "s_and_b32 s21, s21, 0xffff\n\t"
      "s_mov_b32 s22, 2359296\n\t"
      "s_mov_b32 s23, 0x20000\n\t"
      "s_add_u32 m0, s14, 0\n\t"
      "s_nop 0\n\t"
      "buffer_load_dwordx4 v1, s[20:23], s40 offen lds\n\t"
      "buffer_load_dwordx4 v233, s[20:23], s40 offen offset:1024 lds\n\t"
      "buffer_load_dwordx4 v234, s[20:23], s40 offen offset:2048 lds\n\t"
      "buffer_load_dwordx4 v235, s[20:23], s40 offen offset:3072 lds\n\t"
      "s_add_u32 m0, s14, 4096\n\t"
      "s_nop 0\n\t"
      "buffer_load_dwordx4 v2, s[20:23], s40 offen lds\n\t"
      "buffer_load_dwordx4 v237, s[20:23], s40 offen offset:1024 lds\n\t"
      "buffer_load_dwordx4 v238, s[20:23], s40 offen offset:2048 lds\n\t"
      "buffer_load_dwordx4 v239, s[20:23], s40 offen offset:3072 lds\n\t"
      "s_setprio 0\n\t"
      "s_add_u32 s50, s40, 128\n\t"
      "s_cmp_ge_u32 s50, 1152\n\t"
      "s_cselect_b32 s41, 0, s50\n\t"
      "s_add_u32 m0, s14, 32768\n\t"
      "s_nop 0\n\t"
      "buffer_load_dwordx4 v1, s[20:23], s41 offen lds\n\t"
      "buffer_load_dwordx4 v233, s[20:23], s41 offen offset:1024 lds\n\t"
      "buffer_load_dwordx4 v234, s[20:23], s41 offen offset:2048 lds\n\t"
      "buffer_load_dwordx4 v235, s[20:23], s41 offen offset:3072 lds\n\t"
      "s_add_u32 m0, s14, 36864\n\t"
      "s_nop 0\n\t"
      "buffer_load_dwordx4 v2, s[20:23], s41 offen lds\n\t"
      "buffer_load_dwordx4 v237, s[20:23], s41 offen offset:1024 lds\n\t"
      "buffer_load_dwordx4 v238, s[20:23], s41 offen offset:2048 lds\n\t"
      "buffer_load_dwordx4 v239, s[20:23], s41 offen offset:3072 lds\n\t"
      "v_and_b32 v11, 15, v0\n\t"
      "v_bfe_u32 v12, v0, 4, 2\n\t"
      "v_and_b32 v13, 7, v0\n\t"
      "v_xor_b32 v3, v12, v13\n\t"
      "v_lshlrev_b32 v3, 4, v3\n\t"
      "v_lshl_add_u32 v3, v11, 7, v3\n\t"
      "v_add_u32 v3, s14, v3\n\t"
      "v_xor_b32 v4, 64, v3\n\t"
      "v_add_u32 v5, 0x10000, v3\n\t"
      "v_add_u32 v6, 0x10000, v4\n\t"
      "v_and_b32 v7, 63, v0\n\t"
      "v_lshlrev_b32 v7, 4, v7\n\t"
      "v_add_u32 v7, %[lds], v7\n\t"
      "v_mul_u32_u24 v12, 36864, v12\n\t"
      "v_lshl_add_u32 v8, v11, 3, v12\n\t"
      "s_mul_i32 s50, s17, 9216\n\t"
      "s_mul_i32 s51, s18, 9216\n\t"
      "s_lshl_b32 s53, s12, 1\n\t"
      "s_add_u32 s51, s51, s53\n\t"
      "s_add_u32 s53, s50, s53\n\t"
      "v_add_u32 v9, s51, v8\n\t"
      "v_add_u32 v8, s53, v8\n\t"
      "s_and_b32 s51, s11, 1\n\t"
      "s_lshl_b32 s51, s51, 5\n\t"
      "s_add_u32 s51, s51, s13\n\t"
      "s_add_u32 s51, s51, 512\n\t"
      "s_lshl_b32 s51, s51, 1\n\t"
      "s_add_u32 s50, s50, s51\n\t"
      "v_lshl_add_u32 v10, v13, 3, v12\n\t"
      "v_bfe_u32 v11, v0, 3, 1\n\t"
      "v_lshl_add_u32 v10, v11, 2, v10\n\t"
      "v_add_u32 v10, s50, v10\n\t"
      "s_and_b32 s37, s37, 0xffff\n\t"
      "s_mov_b32 s38, 18874368\n\t"
      "s_mov_b32 s39, 0x20000\n\t"
      "s_mov_b32 s60, 0\n\t"
      "s_mov_b32 s61, 9216\n\t"
      "s_mov_b32 s62, 18432\n\t"
      "s_mov_b32 s63, 27648\n\t"
      "s_mov_b32 s64, 147456\n\t"
      "s_mov_b32 s65, 156672\n\t"
      "s_mov_b32 s66, 165888\n\t"
      "s_mov_b32 s67, 175104\n\t"
      "s_waitcnt vmcnt(8)\n\t"
      "s_barrier\n\t"
      "s_setprio 1\n\t"
      "ds_read_b128 v[40:43], v3 offset:0\n\t"
      "ds_read_b128 v[44:47], v3 offset:2048\n\t"
      "ds_read_b128 v[48:51], v3 offset:4096\n\t"
      "ds_read_b128 v[52:55], v3 offset:6144\n\t"
      "ds_read_b128 v[16:19], v7 offset:0\n\t"
      "ds_read_b128 v[20:23], v7 offset:1024\n\t"
      "ds_read_b128 v[24:27], v7 offset:2048\n\t"
      "ds_read_b128 v[28:31], v7 offset:3072\n\t"
      "s_waitcnt lgkmcnt(3)\n\t"
      "v_mfma_f32_16x16x32_bf16 v[72:75], v[40:43], v[16:19], 0\n\t"
      "v_mfma_f32_16x16x32_bf16 v[104:107], v[44:47], v[16:19], 0\n\t"
      "ds_read_b128 v[56:59], v4 offset:0\n\t"
      "v_mfma_f32_16x16x32_bf16 v[136:139], v[48:51], v[16:19], 0\n\t"
      "v_mfma_f32_16x16x32_bf16 v[168:171], v[52:55], v[16:19], 0\n\t"
      "ds_read_b128 v[60:63], v4 offset:2048\n\t"
      "ds_read_b128 v[32:35], v7 offset:4096\n\t"
      "s_waitcnt lgkmcnt(5)\n\t"
      "v_mfma_f32_16x16x32_bf16 v[76:79], v[40:43], v[20:23], 0\n\t"
      "v_mfma_f32_16x16x32_bf16 v[108:111], v[44:47], v[20:23], 0\n\t"
      "ds_read_b128 v[64:67], v4 offset:4096\n\t"
      "v_mfma_f32_16x16x32_bf16 v[140:143], v[48:51], v[20:23], 0\n\t"
      "v_mfma_f32_16x16x32_bf16 v[172:175], v[52:55], v[20:23], 0\n\t"
      "ds_read_b128 v[68:71], v4 offset:6144\n\t"
      "ds_read_b128 v[36:39], v7 offset:5120\n\t"
      "s_waitcnt lgkmcnt(7)\n\t"
      "v_mfma_f32_16x16x32_bf16 v[80:83], v[40:43], v[24:27], 0\n\t"
      "v_mfma_f32_16x16x32_bf16 v[112:115], v[44:47], v[24:27], 0\n\t"
      "v_mfma_f32_16x16x32_bf16 v[144:147], v[48:51], v[24:27], 0\n\t"
      "v_mfma_f32_16x16x32_bf16 v[176:179], v[52:55], v[24:27], 0\n\t"
      "ds_read_b128 v[16:19], v7 offset:6144\n\t"
      "s_waitcnt lgkmcnt(7)\n\t"
      "v_mfma_f32_16x16x32_bf16 v[84:87], v[40:43], v[28:31], 0\n\t"
      "v_mfma_f32_16x16x32_bf16 v[116:119], v[44:47], v[28:31], 0\n\t"
      "v_mfma_f32_16x16x32_bf16 v[148:151], v[48:51], v[28:31], 0\n\t"
      "v_mfma_f32_16x16x32_bf16 v[180:183], v[52:55], v[28:31], 0\n\t"
      "ds_read_b128 v[20:23], v7 offset:7168\n\t"
      "s_waitcnt lgkmcnt(5)\n\t"
      "v_mfma_f32_16x16x32_bf16 v[88:91], v[40:43], v[32:35], 0\n\t"
      "v_mfma_f32_16x16x32_bf16 v[120:123], v[44:47], v[32:35], 0\n\t"
      "v_mfma_f32_16x16x32_bf16 v[152:155], v[48:51], v[32:35], 0\n\t"
      "v_mfma_f32_16x16x32_bf16 v[184:187], v[52:55], v[32:35], 0\n\t"
      "ds_read_b128 v[24:27], v7 offset:16384\n\t"
      "s_waitcnt lgkmcnt(3)\n\t"
      "v_mfma_f32_16x16x32_bf16 v[92:95], v[40:43], v[36:39], 0\n\t"
      "v_mfma_f32_16x16x32_bf16 v[124:127], v[44:47], v[36:39], 0\n\t"
      "v_mfma_f32_16x16x32_bf16 v[156:159], v[48:51], v[36:39], 0\n\t"
      "v_mfma_f32_16x16x32_bf16 v[188:191], v[52:55], v[36:39], 0\n\t"
      "ds_read_b128 v[28:31], v7 offset:17408\n\t"
      "s_waitcnt lgkmcnt(3)\n\t"
      "v_mfma_f32_16x16x32_bf16 v[96:99], v[40:43], v[16:19], 0\n\t"
      "v_mfma_f32_16x16x32_bf16 v[128:131], v[44:47], v[16:19], 0\n\t"
      "v_mfma_f32_16x16x32_bf16 v[160:163], v[48:51], v[16:19], 0\n\t"
      "v_mfma_f32_16x16x32_bf16 v[192:195], v[52:55], v[16:19], 0\n\t"
      "ds_read_b128 v[32:35], v7 offset:8192\n\t"
      "s_waitcnt lgkmcnt(3)\n\t"
      "v_mfma_f32_16x16x32_bf16 v[100:103], v[40:43], v[20:23], 0\n\t"
      "v_mfma_f32_16x16x32_bf16 v[132:135], v[44:47], v[20:23], 0\n\t"
      "v_mfma_f32_16x16x32_bf16 v[164:167], v[48:51], v[20:23], 0\n\t"
      "v_mfma_f32_16x16x32_bf16 v[196:199], v[52:55], v[20:23], 0\n\t"
      "ds_read_b128 v[36:39], v7 offset:9216\n\t"
      "s_waitcnt lgkmcnt(3)\n\t"
      "v_mfma_f32_16x16x32_bf16 v[200:203], v[40:43], v[24:27], 0\n\t"
      "v_mfma_f32_16x16x32_bf16 v[208:211], v[44:47], v[24:27], 0\n\t"
      "ds_read_b128 v[16:19], v7 offset:10240\n\t"
      "s_waitcnt lgkmcnt(3)\n\t"
      "v_mfma_f32_16x16x32_bf16 v[204:207], v[40:43], v[28:31], 0\n\t"
      "v_mfma_f32_16x16x32_bf16 v[212:215], v[44:47], v[28:31], 0\n\t"
      "ds_read_b128 v[20:23], v7 offset:11264\n\t"
      "s_waitcnt lgkmcnt(3)\n\t"
      "v_mfma_f32_16x16x32_bf16 v[72:75], v[56:59], v[32:35], v[72:75]\n\t"
      "v_mfma_f32_16x16x32_bf16 v[104:107], v[60:63], v[32:35], v[104:107]\n\t"
      "v_mfma_f32_16x16x32_bf16 v[136:139], v[64:67], v[32:35], v[136:139]\n\t"
      "v_mfma_f32_16x16x32_bf16 v[168:171], v[68:71], v[32:35], v[168:171]\n\t"
      "ds_read_b128 v[24:27], v7 offset:12288\n\t"
      "s_waitcnt lgkmcnt(3)\n\t"
      "v_mfma_f32_16x16x32_bf16 v[76:79], v[56:59], v[36:39], v[76:79]\n\t"
      "v_mfma_f32_16x16x32_bf16 v[108:111], v[60:63], v[36:39], v[108:111]\n\t"
      "v_mfma_f32_16x16x32_bf16 v[140:143], v[64:67], v[36:39], v[140:143]\n\t"
      "v_mfma_f32_16x16x32_bf16 v[172:175], v[68:71], v[36:39], v[172:175]\n\t"
      "ds_read_b128 v[28:31], v7 offset:13312\n\t"
      "s_waitcnt lgkmcnt(3)\n\t"
      "v_mfma_f32_16x16x32_bf16 v[80:83], v[56:59], v[16:19], v[80:83]\n\t"
      "v_mfma_f32_16x16x32_bf16 v[112:115], v[60:63], v[16:19], v[112:115]\n\t"
      "v_mfma_f32_16x16x32_bf16 v[144:147], v[64:67], v[16:19], v[144:147]\n\t"
      "v_mfma_f32_16x16x32_bf16 v[176:179], v[68:71], v[16:19], v[176:179]\n\t"
      "ds_read_b128 v[32:35], v7 offset:14336\n\t"
      "s_waitcnt lgkmcnt(3)\n\t"
      "v_mfma_f32_16x16x32_bf16 v[84:87], v[56:59], v[20:23], v[84:87]\n\t"
      "v_mfma_f32_16x16x32_bf16 v[116:119], v[60:63], v[20:23], v[116:119]\n\t"
      "v_mfma_f32_16x16x32_bf16 v[148:151], v[64:67], v[20:23], v[148:151]\n\t"
      "v_mfma_f32_16x16x32_bf16 v[180:183], v[68:71], v[20:23], v[180:183]\n\t"
      "ds_read_b128 v[36:39], v7 offset:15360\n\t"
      "s_waitcnt lgkmcnt(3)\n\t"
      "v_mfma_f32_16x16x32_bf16 v[88:91], v[56:59], v[24:27], v[88:91]\n\t"
      "v_mfma_f32_16x16x32_bf16 v[120:123], v[60:63], v[24:27], v[120:123]\n\t"
      "v_mfma_f32_16x16x32_bf16 v[152:155], v[64:67], v[24:27], v[152:155]\n\t"
      "v_mfma_f32_16x16x32_bf16 v[184:187], v[68:71], v[24:27], v[184:187]\n\t"
      "ds_read_b128 v[16:19], v7 offset:18432\n\t"
      "s_waitcnt lgkmcnt(3)\n\t"
      "v_mfma_f32_16x16x32_bf16 v[92:95], v[56:59], v[28:31], v[92:95]\n\t"
      "v_mfma_f32_16x16x32_bf16 v[124:127], v[60:63], v[28:31], v[124:127]\n\t"
      "v_mfma_f32_16x16x32_bf16 v[156:159], v[64:67], v[28:31], v[156:159]\n\t"
      "v_mfma_f32_16x16x32_bf16 v[188:191], v[68:71], v[28:31], v[188:191]\n\t"
      "ds_read_b128 v[20:23], v7 offset:19456\n\t"
      "s_waitcnt vmcnt(0)\n\t"
      "s_waitcnt lgkmcnt(0)\n\t"
      "s_barrier\n\t"
      "v_mfma_f32_16x16x32_bf16 v[96:99], v[56:59], v[32:35], v[96:99]\n\t"
      "v_mfma_f32_16x16x32_bf16 v[128:131], v[60:63], v[32:35], v[128:131]\n\t"
      "ds_read_b128 v[40:43], v3 offset:32768\n\t"
      "v_mfma_f32_16x16x32_bf16 v[160:163], v[64:67], v[32:35], v[160:163]\n\t"
      "v_mfma_f32_16x16x32_bf16 v[192:195], v[68:71], v[32:35], v[192:195]\n\t"
      "ds_read_b128 v[44:47], v3 offset:34816\n\t"
      "ds_read_b128 v[24:27], v7 offset:20480\n\t"
      "v_mfma_f32_16x16x32_bf16 v[100:103], v[56:59], v[36:39], v[100:103]\n\t"
      "v_mfma_f32_16x16x32_bf16 v[132:135], v[60:63], v[36:39], v[132:135]\n\t"
      "ds_read_b128 v[48:51], v3 offset:36864\n\t"
      "v_mfma_f32_16x16x32_bf16 v[164:167], v[64:67], v[36:39], v[164:167]\n\t"
      "v_mfma_f32_16x16x32_bf16 v[196:199], v[68:71], v[36:39], v[196:199]\n\t"
      "ds_read_b128 v[52:55], v3 offset:38912\n\t"
      "ds_read_b128 v[28:31], v7 offset:21504\n\t"
      "v_mfma_f32_16x16x32_bf16 v[200:203], v[56:59], v[16:19], v[200:203]\n\t"
      "v_mfma_f32_16x16x32_bf16 v[208:211], v[60:63], v[16:19], v[208:211]\n\t"
      "ds_read_b128 v[32:35], v7 offset:22528\n\t"
      "v_mfma_f32_16x16x32_bf16 v[204:207], v[56:59], v[20:23], v[204:207]\n\t"
      "v_mfma_f32_16x16x32_bf16 v[212:215], v[60:63], v[20:23], v[212:215]\n\t"
      "ds_read_b128 v[36:39], v7 offset:23552\n\t"
      "s_waitcnt lgkmcnt(5)\n\t"
      "v_mfma_f32_16x16x32_bf16 v[72:75], v[40:43], v[24:27], v[72:75]\n\t"
      "v_mfma_f32_16x16x32_bf16 v[104:107], v[44:47], v[24:27], v[104:107]\n\t"
      "ds_read_b128 v[56:59], v4 offset:32768\n\t"
      "s_waitcnt lgkmcnt(5)\n\t"
      "v_mfma_f32_16x16x32_bf16 v[136:139], v[48:51], v[24:27], v[136:139]\n\t"
      "s_waitcnt lgkmcnt(4)\n\t"
      "v_mfma_f32_16x16x32_bf16 v[168:171], v[52:55], v[24:27], v[168:171]\n\t"
      "ds_read_b128 v[60:63], v4 offset:34816\n\t"
      "ds_read_b128 v[16:19], v7 offset:24576\n\t"
      "s_waitcnt lgkmcnt(5)\n\t"
      "v_mfma_f32_16x16x32_bf16 v[76:79], v[40:43], v[28:31], v[76:79]\n\t"
      "v_mfma_f32_16x16x32_bf16 v[108:111], v[44:47], v[28:31], v[108:111]\n\t"
      "ds_read_b128 v[64:67], v4 offset:36864\n\t"
      "v_mfma_f32_16x16x32_bf16 v[140:143], v[48:51], v[28:31], v[140:143]\n\t"
      "v_mfma_f32_16x16x32_bf16 v[172:175], v[52:55], v[28:31], v[172:175]\n\t"
      "ds_read_b128 v[68:71], v4 offset:38912\n\t"
      "ds_read_b128 v[20:23], v7 offset:25600\n\t"
      "s_waitcnt lgkmcnt(7)\n\t"
      "v_mfma_f32_16x16x32_bf16 v[80:83], v[40:43], v[32:35], v[80:83]\n\t"
      "v_mfma_f32_16x16x32_bf16 v[112:115], v[44:47], v[32:35], v[112:115]\n\t"
      "v_mfma_f32_16x16x32_bf16 v[144:147], v[48:51], v[32:35], v[144:147]\n\t"
      "v_mfma_f32_16x16x32_bf16 v[176:179], v[52:55], v[32:35], v[176:179]\n\t"
      "ds_read_b128 v[24:27], v7 offset:26624\n\t"
      "s_waitcnt lgkmcnt(7)\n\t"
      "v_mfma_f32_16x16x32_bf16 v[84:87], v[40:43], v[36:39], v[84:87]\n\t"
      "v_mfma_f32_16x16x32_bf16 v[116:119], v[44:47], v[36:39], v[116:119]\n\t"
      "v_mfma_f32_16x16x32_bf16 v[148:151], v[48:51], v[36:39], v[148:151]\n\t"
      "v_mfma_f32_16x16x32_bf16 v[180:183], v[52:55], v[36:39], v[180:183]\n\t"
      "ds_read_b128 v[28:31], v7 offset:27648\n\t"
      "s_waitcnt lgkmcnt(5)\n\t"
      "v_mfma_f32_16x16x32_bf16 v[88:91], v[40:43], v[16:19], v[88:91]\n\t"
      "v_mfma_f32_16x16x32_bf16 v[120:123], v[44:47], v[16:19], v[120:123]\n\t"
      "v_mfma_f32_16x16x32_bf16 v[152:155], v[48:51], v[16:19], v[152:155]\n\t"
      "v_mfma_f32_16x16x32_bf16 v[184:187], v[52:55], v[16:19], v[184:187]\n\t"
      "ds_read_b128 v[32:35], v7 offset:36864\n\t"
      "s_waitcnt lgkmcnt(3)\n\t"
      "v_mfma_f32_16x16x32_bf16 v[92:95], v[40:43], v[20:23], v[92:95]\n\t"
      "v_mfma_f32_16x16x32_bf16 v[124:127], v[44:47], v[20:23], v[124:127]\n\t"
      "v_mfma_f32_16x16x32_bf16 v[156:159], v[48:51], v[20:23], v[156:159]\n\t"
      "v_mfma_f32_16x16x32_bf16 v[188:191], v[52:55], v[20:23], v[188:191]\n\t"
      "ds_read_b128 v[36:39], v7 offset:37888\n\t"
      "s_waitcnt lgkmcnt(3)\n\t"
      "v_mfma_f32_16x16x32_bf16 v[96:99], v[40:43], v[24:27], v[96:99]\n\t"
      "v_mfma_f32_16x16x32_bf16 v[128:131], v[44:47], v[24:27], v[128:131]\n\t"
      "v_mfma_f32_16x16x32_bf16 v[160:163], v[48:51], v[24:27], v[160:163]\n\t"
      "v_mfma_f32_16x16x32_bf16 v[192:195], v[52:55], v[24:27], v[192:195]\n\t"
      "ds_read_b128 v[16:19], v7 offset:28672\n\t"
      "s_waitcnt lgkmcnt(3)\n\t"
      "v_mfma_f32_16x16x32_bf16 v[100:103], v[40:43], v[28:31], v[100:103]\n\t"
      "v_mfma_f32_16x16x32_bf16 v[132:135], v[44:47], v[28:31], v[132:135]\n\t"
      "v_mfma_f32_16x16x32_bf16 v[164:167], v[48:51], v[28:31], v[164:167]\n\t"
      "v_mfma_f32_16x16x32_bf16 v[196:199], v[52:55], v[28:31], v[196:199]\n\t"
      "ds_read_b128 v[20:23], v7 offset:29696\n\t"
      "s_waitcnt lgkmcnt(3)\n\t"
      "v_mfma_f32_16x16x32_bf16 v[200:203], v[40:43], v[32:35], v[200:203]\n\t"
      "v_mfma_f32_16x16x32_bf16 v[208:211], v[44:47], v[32:35], v[208:211]\n\t"
      "ds_read_b128 v[24:27], v7 offset:30720\n\t"
      "s_waitcnt lgkmcnt(3)\n\t"
      "v_mfma_f32_16x16x32_bf16 v[204:207], v[40:43], v[36:39], v[204:207]\n\t"
      "v_mfma_f32_16x16x32_bf16 v[212:215], v[44:47], v[36:39], v[212:215]\n\t"
      "ds_read_b128 v[28:31], v7 offset:31744\n\t"
      "s_waitcnt lgkmcnt(3)\n\t"
      "v_mfma_f32_16x16x32_bf16 v[72:75], v[56:59], v[16:19], v[72:75]\n\t"
      "v_mfma_f32_16x16x32_bf16 v[104:107], v[60:63], v[16:19], v[104:107]\n\t"
      "v_mfma_f32_16x16x32_bf16 v[136:139], v[64:67], v[16:19], v[136:139]\n\t"
      "v_mfma_f32_16x16x32_bf16 v[168:171], v[68:71], v[16:19], v[168:171]\n\t"
      "ds_read_b128 v[32:35], v7 offset:32768\n\t"
      "s_waitcnt lgkmcnt(3)\n\t"
      "v_mfma_f32_16x16x32_bf16 v[76:79], v[56:59], v[20:23], v[76:79]\n\t"
      "v_mfma_f32_16x16x32_bf16 v[108:111], v[60:63], v[20:23], v[108:111]\n\t"
      "v_mfma_f32_16x16x32_bf16 v[140:143], v[64:67], v[20:23], v[140:143]\n\t"
      "v_mfma_f32_16x16x32_bf16 v[172:175], v[68:71], v[20:23], v[172:175]\n\t"
      "ds_read_b128 v[36:39], v7 offset:33792\n\t"
      "s_waitcnt lgkmcnt(3)\n\t"
      "v_mfma_f32_16x16x32_bf16 v[80:83], v[56:59], v[24:27], v[80:83]\n\t"
      "v_mfma_f32_16x16x32_bf16 v[112:115], v[60:63], v[24:27], v[112:115]\n\t"
      "v_mfma_f32_16x16x32_bf16 v[144:147], v[64:67], v[24:27], v[144:147]\n\t"
      "v_mfma_f32_16x16x32_bf16 v[176:179], v[68:71], v[24:27], v[176:179]\n\t"
      "ds_read_b128 v[16:19], v7 offset:34816\n\t"
      "s_waitcnt lgkmcnt(3)\n\t"
      "v_mfma_f32_16x16x32_bf16 v[84:87], v[56:59], v[28:31], v[84:87]\n\t"
      "v_mfma_f32_16x16x32_bf16 v[116:119], v[60:63], v[28:31], v[116:119]\n\t"
      "v_mfma_f32_16x16x32_bf16 v[148:151], v[64:67], v[28:31], v[148:151]\n\t"
      "v_mfma_f32_16x16x32_bf16 v[180:183], v[68:71], v[28:31], v[180:183]\n\t"
      "ds_read_b128 v[20:23], v7 offset:35840\n\t"
      "s_waitcnt lgkmcnt(3)\n\t"
      "v_mfma_f32_16x16x32_bf16 v[88:91], v[56:59], v[32:35], v[88:91]\n\t"
      "v_mfma_f32_16x16x32_bf16 v[120:123], v[60:63], v[32:35], v[120:123]\n\t"
      "v_mfma_f32_16x16x32_bf16 v[152:155], v[64:67], v[32:35], v[152:155]\n\t"
      "v_mfma_f32_16x16x32_bf16 v[184:187], v[68:71], v[32:35], v[184:187]\n\t"
      "ds_read_b128 v[24:27], v7 offset:38912\n\t"
      "s_waitcnt lgkmcnt(3)\n\t"
      "v_mfma_f32_16x16x32_bf16 v[92:95], v[56:59], v[36:39], v[92:95]\n\t"
      "v_mfma_f32_16x16x32_bf16 v[124:127], v[60:63], v[36:39], v[124:127]\n\t"
      "v_mfma_f32_16x16x32_bf16 v[156:159], v[64:67], v[36:39], v[156:159]\n\t"
      "v_mfma_f32_16x16x32_bf16 v[188:191], v[68:71], v[36:39], v[188:191]\n\t"
      "ds_read_b128 v[28:31], v7 offset:39936\n\t"
      "s_waitcnt lgkmcnt(0)\n\t"
      "s_barrier\n\t"
      "v_mfma_f32_16x16x32_bf16 v[96:99], v[56:59], v[16:19], v[96:99]\n\t"
      "v_mfma_f32_16x16x32_bf16 v[128:131], v[60:63], v[16:19], v[128:131]\n\t"
      "ds_read_b128 v[40:43], v5 offset:0\n\t"
      "v_mfma_f32_16x16x32_bf16 v[160:163], v[64:67], v[16:19], v[160:163]\n\t"
      "v_mfma_f32_16x16x32_bf16 v[192:195], v[68:71], v[16:19], v[192:195]\n\t"
      "ds_read_b128 v[44:47], v5 offset:2048\n\t"
      "ds_read_b128 v[32:35], v7 offset:40960\n\t"
      "v_mfma_f32_16x16x32_bf16 v[100:103], v[56:59], v[20:23], v[100:103]\n\t"
      "v_mfma_f32_16x16x32_bf16 v[132:135], v[60:63], v[20:23], v[132:135]\n\t"
      "ds_read_b128 v[48:51], v5 offset:4096\n\t"
      "v_mfma_f32_16x16x32_bf16 v[164:167], v[64:67], v[20:23], v[164:167]\n\t"
      "v_mfma_f32_16x16x32_bf16 v[196:199], v[68:71], v[20:23], v[196:199]\n\t"
      "ds_read_b128 v[52:55], v5 offset:6144\n\t"
      "ds_read_b128 v[36:39], v7 offset:41984\n\t"
      "v_mfma_f32_16x16x32_bf16 v[200:203], v[56:59], v[24:27], v[200:203]\n\t"
      "v_mfma_f32_16x16x32_bf16 v[208:211], v[60:63], v[24:27], v[208:211]\n\t"
      "ds_read_b128 v[16:19], v7 offset:43008\n\t"
      "v_mfma_f32_16x16x32_bf16 v[204:207], v[56:59], v[28:31], v[204:207]\n\t"
      "v_mfma_f32_16x16x32_bf16 v[212:215], v[60:63], v[28:31], v[212:215]\n\t"
      "ds_read_b128 v[20:23], v7 offset:44032\n\t"
      "s_waitcnt lgkmcnt(5)\n\t"
      "v_mfma_f32_16x16x32_bf16 v[72:75], v[40:43], v[32:35], v[72:75]\n\t"
      "v_mfma_f32_16x16x32_bf16 v[104:107], v[44:47], v[32:35], v[104:107]\n\t"
      "ds_read_b128 v[56:59], v6 offset:0\n\t"
      "s_waitcnt lgkmcnt(5)\n\t"
      "v_mfma_f32_16x16x32_bf16 v[136:139], v[48:51], v[32:35], v[136:139]\n\t"
      "s_waitcnt lgkmcnt(4)\n\t"
      "v_mfma_f32_16x16x32_bf16 v[168:171], v[52:55], v[32:35], v[168:171]\n\t"
      "ds_read_b128 v[60:63], v6 offset:2048\n\t"
      "ds_read_b128 v[24:27], v7 offset:45056\n\t"
      "s_waitcnt lgkmcnt(5)\n\t"
      "v_mfma_f32_16x16x32_bf16 v[76:79], v[40:43], v[36:39], v[76:79]\n\t"
      "v_mfma_f32_16x16x32_bf16 v[108:111], v[44:47], v[36:39], v[108:111]\n\t"
      "ds_read_b128 v[64:67], v6 offset:4096\n\t"
      "v_mfma_f32_16x16x32_bf16 v[140:143], v[48:51], v[36:39], v[140:143]\n\t"
      "v_mfma_f32_16x16x32_bf16 v[172:175], v[52:55], v[36:39], v[172:175]\n\t"
      "ds_read_b128 v[68:71], v6 offset:6144\n\t"
      "ds_read_b128 v[28:31], v7 offset:46080\n\t"
      "s_waitcnt lgkmcnt(7)\n\t"
      "v_mfma_f32_16x16x32_bf16 v[80:83], v[40:43], v[16:19], v[80:83]\n\t"
      "v_mfma_f32_16x16x32_bf16 v[112:115], v[44:47], v[16:19], v[112:115]\n\t"
      "v_mfma_f32_16x16x32_bf16 v[144:147], v[48:51], v[16:19], v[144:147]\n\t"
      "v_mfma_f32_16x16x32_bf16 v[176:179], v[52:55], v[16:19], v[176:179]\n\t"
      "ds_read_b128 v[32:35], v7 offset:47104\n\t"
      "s_waitcnt lgkmcnt(7)\n\t"
      "v_mfma_f32_16x16x32_bf16 v[84:87], v[40:43], v[20:23], v[84:87]\n\t"
      "v_mfma_f32_16x16x32_bf16 v[116:119], v[44:47], v[20:23], v[116:119]\n\t"
      "v_mfma_f32_16x16x32_bf16 v[148:151], v[48:51], v[20:23], v[148:151]\n\t"
      "v_mfma_f32_16x16x32_bf16 v[180:183], v[52:55], v[20:23], v[180:183]\n\t"
      "ds_read_b128 v[36:39], v7 offset:48128\n\t"
      "s_waitcnt lgkmcnt(5)\n\t"
      "v_mfma_f32_16x16x32_bf16 v[88:91], v[40:43], v[24:27], v[88:91]\n\t"
      "v_mfma_f32_16x16x32_bf16 v[120:123], v[44:47], v[24:27], v[120:123]\n\t"
      "v_mfma_f32_16x16x32_bf16 v[152:155], v[48:51], v[24:27], v[152:155]\n\t"
      "v_mfma_f32_16x16x32_bf16 v[184:187], v[52:55], v[24:27], v[184:187]\n\t"
      "ds_read_b128 v[16:19], v7 offset:57344\n\t"
      "s_waitcnt lgkmcnt(3)\n\t"
      "v_mfma_f32_16x16x32_bf16 v[92:95], v[40:43], v[28:31], v[92:95]\n\t"
      "v_mfma_f32_16x16x32_bf16 v[124:127], v[44:47], v[28:31], v[124:127]\n\t"
      "v_mfma_f32_16x16x32_bf16 v[156:159], v[48:51], v[28:31], v[156:159]\n\t"
      "v_mfma_f32_16x16x32_bf16 v[188:191], v[52:55], v[28:31], v[188:191]\n\t"
      "ds_read_b128 v[20:23], v7 offset:58368\n\t"
      "s_waitcnt lgkmcnt(3)\n\t"
      "v_mfma_f32_16x16x32_bf16 v[96:99], v[40:43], v[32:35], v[96:99]\n\t"
      "v_mfma_f32_16x16x32_bf16 v[128:131], v[44:47], v[32:35], v[128:131]\n\t"
      "v_mfma_f32_16x16x32_bf16 v[160:163], v[48:51], v[32:35], v[160:163]\n\t"
      "v_mfma_f32_16x16x32_bf16 v[192:195], v[52:55], v[32:35], v[192:195]\n\t"
      "ds_read_b128 v[24:27], v7 offset:49152\n\t"
      "s_waitcnt lgkmcnt(3)\n\t"
      "v_mfma_f32_16x16x32_bf16 v[100:103], v[40:43], v[36:39], v[100:103]\n\t"
      "v_mfma_f32_16x16x32_bf16 v[132:135], v[44:47], v[36:39], v[132:135]\n\t"
      "v_mfma_f32_16x16x32_bf16 v[164:167], v[48:51], v[36:39], v[164:167]\n\t"
      "v_mfma_f32_16x16x32_bf16 v[196:199], v[52:55], v[36:39], v[196:199]\n\t"
      "ds_read_b128 v[28:31], v7 offset:50176\n\t"
      "s_waitcnt lgkmcnt(3)\n\t"
      "v_mfma_f32_16x16x32_bf16 v[200:203], v[40:43], v[16:19], v[200:203]\n\t"
      "v_mfma_f32_16x16x32_bf16 v[208:211], v[44:47], v[16:19], v[208:211]\n\t"
      "ds_read_b128 v[32:35], v7 offset:51200\n\t"
      "s_waitcnt lgkmcnt(3)\n\t"
      "v_mfma_f32_16x16x32_bf16 v[204:207], v[40:43], v[20:23], v[204:207]\n\t"
      "v_mfma_f32_16x16x32_bf16 v[212:215], v[44:47], v[20:23], v[212:215]\n\t"
      "ds_read_b128 v[36:39], v7 offset:52224\n\t"
      "s_waitcnt lgkmcnt(3)\n\t"
      "v_mfma_f32_16x16x32_bf16 v[72:75], v[56:59], v[24:27], v[72:75]\n\t"
      "v_mfma_f32_16x16x32_bf16 v[104:107], v[60:63], v[24:27], v[104:107]\n\t"
      "v_mfma_f32_16x16x32_bf16 v[136:139], v[64:67], v[24:27], v[136:139]\n\t"
      "v_mfma_f32_16x16x32_bf16 v[168:171], v[68:71], v[24:27], v[168:171]\n\t"
      "ds_read_b128 v[16:19], v7 offset:53248\n\t"
      "s_waitcnt lgkmcnt(3)\n\t"
      "v_mfma_f32_16x16x32_bf16 v[76:79], v[56:59], v[28:31], v[76:79]\n\t"
      "v_mfma_f32_16x16x32_bf16 v[108:111], v[60:63], v[28:31], v[108:111]\n\t"
      "v_mfma_f32_16x16x32_bf16 v[140:143], v[64:67], v[28:31], v[140:143]\n\t"
      "v_mfma_f32_16x16x32_bf16 v[172:175], v[68:71], v[28:31], v[172:175]\n\t"
      "ds_read_b128 v[20:23], v7 offset:54272\n\t"
      "s_waitcnt lgkmcnt(3)\n\t"
      "v_mfma_f32_16x16x32_bf16 v[80:83], v[56:59], v[32:35], v[80:83]\n\t"
      "v_mfma_f32_16x16x32_bf16 v[112:115], v[60:63], v[32:35], v[112:115]\n\t"
      "v_mfma_f32_16x16x32_bf16 v[144:147], v[64:67], v[32:35], v[144:147]\n\t"
      "v_mfma_f32_16x16x32_bf16 v[176:179], v[68:71], v[32:35], v[176:179]\n\t"
      "ds_read_b128 v[24:27], v7 offset:55296\n\t"
      "s_waitcnt lgkmcnt(3)\n\t"
      "v_mfma_f32_16x16x32_bf16 v[84:87], v[56:59], v[36:39], v[84:87]\n\t"
      "v_mfma_f32_16x16x32_bf16 v[116:119], v[60:63], v[36:39], v[116:119]\n\t"
      "v_mfma_f32_16x16x32_bf16 v[148:151], v[64:67], v[36:39], v[148:151]\n\t"
      "v_mfma_f32_16x16x32_bf16 v[180:183], v[68:71], v[36:39], v[180:183]\n\t"
      "ds_read_b128 v[28:31], v7 offset:56320\n\t"
      "s_waitcnt lgkmcnt(3)\n\t"
      "v_mfma_f32_16x16x32_bf16 v[88:91], v[56:59], v[16:19], v[88:91]\n\t"
      "v_mfma_f32_16x16x32_bf16 v[120:123], v[60:63], v[16:19], v[120:123]\n\t"
      "v_mfma_f32_16x16x32_bf16 v[152:155], v[64:67], v[16:19], v[152:155]\n\t"
      "v_mfma_f32_16x16x32_bf16 v[184:187], v[68:71], v[16:19], v[184:187]\n\t"
      "ds_read_b128 v[32:35], v7 offset:59392\n\t"
      "s_waitcnt lgkmcnt(3)\n\t"
      "v_mfma_f32_16x16x32_bf16 v[92:95], v[56:59], v[20:23], v[92:95]\n\t"
      "v_mfma_f32_16x16x32_bf16 v[124:127], v[60:63], v[20:23], v[124:127]\n\t"
      "v_mfma_f32_16x16x32_bf16 v[156:159], v[64:67], v[20:23], v[156:159]\n\t"
      "v_mfma_f32_16x16x32_bf16 v[188:191], v[68:71], v[20:23], v[188:191]\n\t"
      "ds_read_b128 v[36:39], v7 offset:60416\n\t"
      "s_waitcnt lgkmcnt(0)\n\t"
      "s_barrier\n\t"
      "v_mfma_f32_16x16x32_bf16 v[96:99], v[56:59], v[24:27], v[96:99]\n\t"
      "v_mfma_f32_16x16x32_bf16 v[128:131], v[60:63], v[24:27], v[128:131]\n\t"
      "ds_read_b128 v[40:43], v3 offset:0\n\t"
      "v_mfma_f32_16x16x32_bf16 v[160:163], v[64:67], v[24:27], v[160:163]\n\t"
      "v_mfma_f32_16x16x32_bf16 v[192:195], v[68:71], v[24:27], v[192:195]\n\t"
      "ds_read_b128 v[44:47], v3 offset:2048\n\t"
      "ds_read_b128 v[16:19], v7 offset:0\n\t"
      "v_mfma_f32_16x16x32_bf16 v[100:103], v[56:59], v[28:31], v[100:103]\n\t"
      "v_mfma_f32_16x16x32_bf16 v[132:135], v[60:63], v[28:31], v[132:135]\n\t"
      "ds_read_b128 v[48:51], v3 offset:4096\n\t"
      "v_mfma_f32_16x16x32_bf16 v[164:167], v[64:67], v[28:31], v[164:167]\n\t"
      "v_mfma_f32_16x16x32_bf16 v[196:199], v[68:71], v[28:31], v[196:199]\n\t"
      "ds_read_b128 v[52:55], v3 offset:6144\n\t"
      "ds_read_b128 v[20:23], v7 offset:1024\n\t"
      "v_mfma_f32_16x16x32_bf16 v[200:203], v[56:59], v[32:35], v[200:203]\n\t"
      "v_mfma_f32_16x16x32_bf16 v[208:211], v[60:63], v[32:35], v[208:211]\n\t"
      "ds_read_b128 v[24:27], v7 offset:2048\n\t"
      "v_mfma_f32_16x16x32_bf16 v[204:207], v[56:59], v[36:39], v[204:207]\n\t"
      "v_mfma_f32_16x16x32_bf16 v[212:215], v[60:63], v[36:39], v[212:215]\n\t"
      "ds_read_b128 v[28:31], v7 offset:3072\n\t"
      "s_waitcnt lgkmcnt(5)\n\t"
      "v_mfma_f32_16x16x32_bf16 v[72:75], v[40:43], v[16:19], v[72:75]\n\t"
      "v_mfma_f32_16x16x32_bf16 v[104:107], v[44:47], v[16:19], v[104:107]\n\t"
      "ds_read_b128 v[56:59], v4 offset:0\n\t"
      "s_waitcnt lgkmcnt(5)\n\t"
      "v_mfma_f32_16x16x32_bf16 v[136:139], v[48:51], v[16:19], v[136:139]\n\t"
      "s_waitcnt lgkmcnt(4)\n\t"
      "v_mfma_f32_16x16x32_bf16 v[168:171], v[52:55], v[16:19], v[168:171]\n\t"
      "ds_read_b128 v[60:63], v4 offset:2048\n\t"
      "ds_read_b128 v[32:35], v7 offset:4096\n\t"
      "s_waitcnt lgkmcnt(5)\n\t"
      "v_mfma_f32_16x16x32_bf16 v[76:79], v[40:43], v[20:23], v[76:79]\n\t"
      "v_mfma_f32_16x16x32_bf16 v[108:111], v[44:47], v[20:23], v[108:111]\n\t"
      "ds_read_b128 v[64:67], v4 offset:4096\n\t"
      "v_mfma_f32_16x16x32_bf16 v[140:143], v[48:51], v[20:23], v[140:143]\n\t"
      "v_mfma_f32_16x16x32_bf16 v[172:175], v[52:55], v[20:23], v[172:175]\n\t"
      "ds_read_b128 v[68:71], v4 offset:6144\n\t"
      "ds_read_b128 v[36:39], v7 offset:5120\n\t"
      "s_waitcnt lgkmcnt(7)\n\t"
      "v_mfma_f32_16x16x32_bf16 v[80:83], v[40:43], v[24:27], v[80:83]\n\t"
      "v_mfma_f32_16x16x32_bf16 v[112:115], v[44:47], v[24:27], v[112:115]\n\t"
      "v_mfma_f32_16x16x32_bf16 v[144:147], v[48:51], v[24:27], v[144:147]\n\t"
      "v_mfma_f32_16x16x32_bf16 v[176:179], v[52:55], v[24:27], v[176:179]\n\t"
      "ds_read_b128 v[16:19], v7 offset:6144\n\t"
      "s_waitcnt lgkmcnt(7)\n\t"
      "v_mfma_f32_16x16x32_bf16 v[84:87], v[40:43], v[28:31], v[84:87]\n\t"
      "v_mfma_f32_16x16x32_bf16 v[116:119], v[44:47], v[28:31], v[116:119]\n\t"
      "v_mfma_f32_16x16x32_bf16 v[148:151], v[48:51], v[28:31], v[148:151]\n\t"
      "v_mfma_f32_16x16x32_bf16 v[180:183], v[52:55], v[28:31], v[180:183]\n\t"
      "ds_read_b128 v[20:23], v7 offset:7168\n\t"
      "s_waitcnt lgkmcnt(5)\n\t"
      "v_mfma_f32_16x16x32_bf16 v[88:91], v[40:43], v[32:35], v[88:91]\n\t"
      "v_mfma_f32_16x16x32_bf16 v[120:123], v[44:47], v[32:35], v[120:123]\n\t"
      "v_mfma_f32_16x16x32_bf16 v[152:155], v[48:51], v[32:35], v[152:155]\n\t"
      "v_mfma_f32_16x16x32_bf16 v[184:187], v[52:55], v[32:35], v[184:187]\n\t"
      "ds_read_b128 v[24:27], v7 offset:16384\n\t"
      "s_waitcnt lgkmcnt(3)\n\t"
      "v_mfma_f32_16x16x32_bf16 v[92:95], v[40:43], v[36:39], v[92:95]\n\t"
      "v_mfma_f32_16x16x32_bf16 v[124:127], v[44:47], v[36:39], v[124:127]\n\t"
      "v_mfma_f32_16x16x32_bf16 v[156:159], v[48:51], v[36:39], v[156:159]\n\t"
      "v_mfma_f32_16x16x32_bf16 v[188:191], v[52:55], v[36:39], v[188:191]\n\t"
      "ds_read_b128 v[28:31], v7 offset:17408\n\t"
      "s_waitcnt lgkmcnt(3)\n\t"
      "v_mfma_f32_16x16x32_bf16 v[96:99], v[40:43], v[16:19], v[96:99]\n\t"
      "v_mfma_f32_16x16x32_bf16 v[128:131], v[44:47], v[16:19], v[128:131]\n\t"
      "v_mfma_f32_16x16x32_bf16 v[160:163], v[48:51], v[16:19], v[160:163]\n\t"
      "v_mfma_f32_16x16x32_bf16 v[192:195], v[52:55], v[16:19], v[192:195]\n\t"
      "ds_read_b128 v[32:35], v7 offset:8192\n\t"
      "s_waitcnt lgkmcnt(3)\n\t"
      "v_mfma_f32_16x16x32_bf16 v[100:103], v[40:43], v[20:23], v[100:103]\n\t"
      "v_mfma_f32_16x16x32_bf16 v[132:135], v[44:47], v[20:23], v[132:135]\n\t"
      "v_mfma_f32_16x16x32_bf16 v[164:167], v[48:51], v[20:23], v[164:167]\n\t"
      "v_mfma_f32_16x16x32_bf16 v[196:199], v[52:55], v[20:23], v[196:199]\n\t"
      "ds_read_b128 v[36:39], v7 offset:9216\n\t"
      "s_waitcnt lgkmcnt(3)\n\t"
      "v_mfma_f32_16x16x32_bf16 v[200:203], v[40:43], v[24:27], v[200:203]\n\t"
      "v_mfma_f32_16x16x32_bf16 v[208:211], v[44:47], v[24:27], v[208:211]\n\t"
      "ds_read_b128 v[16:19], v7 offset:10240\n\t"
      "s_waitcnt lgkmcnt(3)\n\t"
      "v_mfma_f32_16x16x32_bf16 v[204:207], v[40:43], v[28:31], v[204:207]\n\t"
      "v_mfma_f32_16x16x32_bf16 v[212:215], v[44:47], v[28:31], v[212:215]\n\t"
      "ds_read_b128 v[20:23], v7 offset:11264\n\t"
      "s_waitcnt lgkmcnt(3)\n\t"
      "v_mfma_f32_16x16x32_bf16 v[72:75], v[56:59], v[32:35], v[72:75]\n\t"
      "v_mfma_f32_16x16x32_bf16 v[104:107], v[60:63], v[32:35], v[104:107]\n\t"
      "v_mfma_f32_16x16x32_bf16 v[136:139], v[64:67], v[32:35], v[136:139]\n\t"
      "v_mfma_f32_16x16x32_bf16 v[168:171], v[68:71], v[32:35], v[168:171]\n\t"
      "ds_read_b128 v[24:27], v7 offset:12288\n\t"
      "s_waitcnt lgkmcnt(3)\n\t"
      "v_mfma_f32_16x16x32_bf16 v[76:79], v[56:59], v[36:39], v[76:79]\n\t"
      "v_mfma_f32_16x16x32_bf16 v[108:111], v[60:63], v[36:39], v[108:111]\n\t"
      "v_mfma_f32_16x16x32_bf16 v[140:143], v[64:67], v[36:39], v[140:143]\n\t"
      "v_mfma_f32_16x16x32_bf16 v[172:175], v[68:71], v[36:39], v[172:175]\n\t"
      "ds_read_b128 v[28:31], v7 offset:13312\n\t"
      "s_waitcnt lgkmcnt(3)\n\t"
      "v_mfma_f32_16x16x32_bf16 v[80:83], v[56:59], v[16:19], v[80:83]\n\t"
      "v_mfma_f32_16x16x32_bf16 v[112:115], v[60:63], v[16:19], v[112:115]\n\t"
      "v_mfma_f32_16x16x32_bf16 v[144:147], v[64:67], v[16:19], v[144:147]\n\t"
      "v_mfma_f32_16x16x32_bf16 v[176:179], v[68:71], v[16:19], v[176:179]\n\t"
      "ds_read_b128 v[32:35], v7 offset:14336\n\t"
      "s_waitcnt lgkmcnt(3)\n\t"
      "v_mfma_f32_16x16x32_bf16 v[84:87], v[56:59], v[20:23], v[84:87]\n\t"
      "v_mfma_f32_16x16x32_bf16 v[116:119], v[60:63], v[20:23], v[116:119]\n\t"
      "v_mfma_f32_16x16x32_bf16 v[148:151], v[64:67], v[20:23], v[148:151]\n\t"
      "v_mfma_f32_16x16x32_bf16 v[180:183], v[68:71], v[20:23], v[180:183]\n\t"
      "ds_read_b128 v[36:39], v7 offset:15360\n\t"
      "s_waitcnt lgkmcnt(3)\n\t"
      "v_mfma_f32_16x16x32_bf16 v[88:91], v[56:59], v[24:27], v[88:91]\n\t"
      "v_mfma_f32_16x16x32_bf16 v[120:123], v[60:63], v[24:27], v[120:123]\n\t"
      "v_mfma_f32_16x16x32_bf16 v[152:155], v[64:67], v[24:27], v[152:155]\n\t"
      "v_mfma_f32_16x16x32_bf16 v[184:187], v[68:71], v[24:27], v[184:187]\n\t"
      "ds_read_b128 v[16:19], v7 offset:18432\n\t"
      "s_waitcnt lgkmcnt(3)\n\t"
      "v_mfma_f32_16x16x32_bf16 v[92:95], v[56:59], v[28:31], v[92:95]\n\t"
      "v_mfma_f32_16x16x32_bf16 v[124:127], v[60:63], v[28:31], v[124:127]\n\t"
      "v_mfma_f32_16x16x32_bf16 v[156:159], v[64:67], v[28:31], v[156:159]\n\t"
      "v_mfma_f32_16x16x32_bf16 v[188:191], v[68:71], v[28:31], v[188:191]\n\t"
      "ds_read_b128 v[20:23], v7 offset:19456\n\t"
      "s_waitcnt lgkmcnt(0)\n\t"
      "s_barrier\n\t"
      "v_mfma_f32_16x16x32_bf16 v[96:99], v[56:59], v[32:35], v[96:99]\n\t"
      "v_mfma_f32_16x16x32_bf16 v[128:131], v[60:63], v[32:35], v[128:131]\n\t"
      "ds_read_b128 v[40:43], v3 offset:32768\n\t"
      "v_mfma_f32_16x16x32_bf16 v[160:163], v[64:67], v[32:35], v[160:163]\n\t"
      "v_mfma_f32_16x16x32_bf16 v[192:195], v[68:71], v[32:35], v[192:195]\n\t"
      "ds_read_b128 v[44:47], v3 offset:34816\n\t"
      "ds_read_b128 v[24:27], v7 offset:20480\n\t"
      "v_mfma_f32_16x16x32_bf16 v[100:103], v[56:59], v[36:39], v[100:103]\n\t"
      "v_mfma_f32_16x16x32_bf16 v[132:135], v[60:63], v[36:39], v[132:135]\n\t"
      "ds_read_b128 v[48:51], v3 offset:36864\n\t"
      "v_mfma_f32_16x16x32_bf16 v[164:167], v[64:67], v[36:39], v[164:167]\n\t"
      "v_mfma_f32_16x16x32_bf16 v[196:199], v[68:71], v[36:39], v[196:199]\n\t"
      "ds_read_b128 v[52:55], v3 offset:38912\n\t"
      "ds_read_b128 v[28:31], v7 offset:21504\n\t"
      "v_mfma_f32_16x16x32_bf16 v[200:203], v[56:59], v[16:19], v[200:203]\n\t"
      "v_mfma_f32_16x16x32_bf16 v[208:211], v[60:63], v[16:19], v[208:211]\n\t"
      "ds_read_b128 v[32:35], v7 offset:22528\n\t"
      "v_mfma_f32_16x16x32_bf16 v[204:207], v[56:59], v[20:23], v[204:207]\n\t"
      "v_mfma_f32_16x16x32_bf16 v[212:215], v[60:63], v[20:23], v[212:215]\n\t"
      "ds_read_b128 v[36:39], v7 offset:23552\n\t"
      "s_waitcnt lgkmcnt(5)\n\t"
      "v_mfma_f32_16x16x32_bf16 v[72:75], v[40:43], v[24:27], v[72:75]\n\t"
      "v_mfma_f32_16x16x32_bf16 v[104:107], v[44:47], v[24:27], v[104:107]\n\t"
      "ds_read_b128 v[56:59], v4 offset:32768\n\t"
      "s_waitcnt lgkmcnt(5)\n\t"
      "v_mfma_f32_16x16x32_bf16 v[136:139], v[48:51], v[24:27], v[136:139]\n\t"
      "s_waitcnt lgkmcnt(4)\n\t"
      "v_mfma_f32_16x16x32_bf16 v[168:171], v[52:55], v[24:27], v[168:171]\n\t"
      "ds_read_b128 v[60:63], v4 offset:34816\n\t"
      "ds_read_b128 v[16:19], v7 offset:24576\n\t"
      "s_waitcnt lgkmcnt(5)\n\t"
      "v_mfma_f32_16x16x32_bf16 v[76:79], v[40:43], v[28:31], v[76:79]\n\t"
      "v_mfma_f32_16x16x32_bf16 v[108:111], v[44:47], v[28:31], v[108:111]\n\t"
      "ds_read_b128 v[64:67], v4 offset:36864\n\t"
      "v_mfma_f32_16x16x32_bf16 v[140:143], v[48:51], v[28:31], v[140:143]\n\t"
      "v_mfma_f32_16x16x32_bf16 v[172:175], v[52:55], v[28:31], v[172:175]\n\t"
      "ds_read_b128 v[68:71], v4 offset:38912\n\t"
      "ds_read_b128 v[20:23], v7 offset:25600\n\t"
      "s_waitcnt lgkmcnt(7)\n\t"
      "v_mfma_f32_16x16x32_bf16 v[80:83], v[40:43], v[32:35], v[80:83]\n\t"
      "v_mfma_f32_16x16x32_bf16 v[112:115], v[44:47], v[32:35], v[112:115]\n\t"
      "v_mfma_f32_16x16x32_bf16 v[144:147], v[48:51], v[32:35], v[144:147]\n\t"
      "v_mfma_f32_16x16x32_bf16 v[176:179], v[52:55], v[32:35], v[176:179]\n\t"
      "ds_read_b128 v[24:27], v7 offset:26624\n\t"
      "s_waitcnt lgkmcnt(7)\n\t"
      "v_mfma_f32_16x16x32_bf16 v[84:87], v[40:43], v[36:39], v[84:87]\n\t"
      "v_mfma_f32_16x16x32_bf16 v[116:119], v[44:47], v[36:39], v[116:119]\n\t"
      "v_mfma_f32_16x16x32_bf16 v[148:151], v[48:51], v[36:39], v[148:151]\n\t"
      "v_mfma_f32_16x16x32_bf16 v[180:183], v[52:55], v[36:39], v[180:183]\n\t"
      "ds_read_b128 v[28:31], v7 offset:27648\n\t"
      "s_waitcnt lgkmcnt(5)\n\t"
      "v_mfma_f32_16x16x32_bf16 v[88:91], v[40:43], v[16:19], v[88:91]\n\t"
      "v_mfma_f32_16x16x32_bf16 v[120:123], v[44:47], v[16:19], v[120:123]\n\t"
      "v_mfma_f32_16x16x32_bf16 v[152:155], v[48:51], v[16:19], v[152:155]\n\t"
      "v_mfma_f32_16x16x32_bf16 v[184:187], v[52:55], v[16:19], v[184:187]\n\t"
      "ds_read_b128 v[32:35], v7 offset:36864\n\t"
      "s_waitcnt lgkmcnt(3)\n\t"
      "v_mfma_f32_16x16x32_bf16 v[92:95], v[40:43], v[20:23], v[92:95]\n\t"
      "v_mfma_f32_16x16x32_bf16 v[124:127], v[44:47], v[20:23], v[124:127]\n\t"
      "v_mfma_f32_16x16x32_bf16 v[156:159], v[48:51], v[20:23], v[156:159]\n\t"
      "v_mfma_f32_16x16x32_bf16 v[188:191], v[52:55], v[20:23], v[188:191]\n\t"
      "ds_read_b128 v[36:39], v7 offset:37888\n\t"
      "s_waitcnt lgkmcnt(3)\n\t"
      "v_mfma_f32_16x16x32_bf16 v[96:99], v[40:43], v[24:27], v[96:99]\n\t"
      "v_mfma_f32_16x16x32_bf16 v[128:131], v[44:47], v[24:27], v[128:131]\n\t"
      "v_mfma_f32_16x16x32_bf16 v[160:163], v[48:51], v[24:27], v[160:163]\n\t"
      "v_mfma_f32_16x16x32_bf16 v[192:195], v[52:55], v[24:27], v[192:195]\n\t"
      "ds_read_b128 v[16:19], v7 offset:28672\n\t"
      "s_waitcnt lgkmcnt(3)\n\t"
      "v_mfma_f32_16x16x32_bf16 v[100:103], v[40:43], v[28:31], v[100:103]\n\t"
      "v_mfma_f32_16x16x32_bf16 v[132:135], v[44:47], v[28:31], v[132:135]\n\t"
      "v_mfma_f32_16x16x32_bf16 v[164:167], v[48:51], v[28:31], v[164:167]\n\t"
      "v_mfma_f32_16x16x32_bf16 v[196:199], v[52:55], v[28:31], v[196:199]\n\t"
      "ds_read_b128 v[20:23], v7 offset:29696\n\t"
      "s_waitcnt lgkmcnt(3)\n\t"
      "v_mfma_f32_16x16x32_bf16 v[200:203], v[40:43], v[32:35], v[200:203]\n\t"
      "v_mfma_f32_16x16x32_bf16 v[208:211], v[44:47], v[32:35], v[208:211]\n\t"
      "ds_read_b128 v[24:27], v7 offset:30720\n\t"
      "s_waitcnt lgkmcnt(3)\n\t"
      "v_mfma_f32_16x16x32_bf16 v[204:207], v[40:43], v[36:39], v[204:207]\n\t"
      "v_mfma_f32_16x16x32_bf16 v[212:215], v[44:47], v[36:39], v[212:215]\n\t"
      "ds_read_b128 v[28:31], v7 offset:31744\n\t"
      "s_waitcnt lgkmcnt(3)\n\t"
      "v_mfma_f32_16x16x32_bf16 v[72:75], v[56:59], v[16:19], v[72:75]\n\t"
      "v_mfma_f32_16x16x32_bf16 v[104:107], v[60:63], v[16:19], v[104:107]\n\t"
      "v_mfma_f32_16x16x32_bf16 v[136:139], v[64:67], v[16:19], v[136:139]\n\t"
      "v_mfma_f32_16x16x32_bf16 v[168:171], v[68:71], v[16:19], v[168:171]\n\t"
      "ds_read_b128 v[32:35], v7 offset:32768\n\t"
      "s_waitcnt lgkmcnt(3)\n\t"
      "v_mfma_f32_16x16x32_bf16 v[76:79], v[56:59], v[20:23], v[76:79]\n\t"
      "v_mfma_f32_16x16x32_bf16 v[108:111], v[60:63], v[20:23], v[108:111]\n\t"
      "v_mfma_f32_16x16x32_bf16 v[140:143], v[64:67], v[20:23], v[140:143]\n\t"
      "v_mfma_f32_16x16x32_bf16 v[172:175], v[68:71], v[20:23], v[172:175]\n\t"
      "ds_read_b128 v[36:39], v7 offset:33792\n\t"
      "s_waitcnt lgkmcnt(3)\n\t"
      "v_mfma_f32_16x16x32_bf16 v[80:83], v[56:59], v[24:27], v[80:83]\n\t"
      "v_mfma_f32_16x16x32_bf16 v[112:115], v[60:63], v[24:27], v[112:115]\n\t"
      "v_mfma_f32_16x16x32_bf16 v[144:147], v[64:67], v[24:27], v[144:147]\n\t"
      "v_mfma_f32_16x16x32_bf16 v[176:179], v[68:71], v[24:27], v[176:179]\n\t"
      "ds_read_b128 v[16:19], v7 offset:34816\n\t"
      "s_waitcnt lgkmcnt(3)\n\t"
      "v_mfma_f32_16x16x32_bf16 v[84:87], v[56:59], v[28:31], v[84:87]\n\t"
      "v_mfma_f32_16x16x32_bf16 v[116:119], v[60:63], v[28:31], v[116:119]\n\t"
      "v_mfma_f32_16x16x32_bf16 v[148:151], v[64:67], v[28:31], v[148:151]\n\t"
      "v_mfma_f32_16x16x32_bf16 v[180:183], v[68:71], v[28:31], v[180:183]\n\t"
      "ds_read_b128 v[20:23], v7 offset:35840\n\t"
      "s_waitcnt lgkmcnt(3)\n\t"
      "v_mfma_f32_16x16x32_bf16 v[88:91], v[56:59], v[32:35], v[88:91]\n\t"
      "v_mfma_f32_16x16x32_bf16 v[120:123], v[60:63], v[32:35], v[120:123]\n\t"
      "v_mfma_f32_16x16x32_bf16 v[152:155], v[64:67], v[32:35], v[152:155]\n\t"
      "v_mfma_f32_16x16x32_bf16 v[184:187], v[68:71], v[32:35], v[184:187]\n\t"
      "ds_read_b128 v[24:27], v7 offset:38912\n\t"
      "s_waitcnt lgkmcnt(3)\n\t"
      "v_mfma_f32_16x16x32_bf16 v[92:95], v[56:59], v[36:39], v[92:95]\n\t"
      "v_mfma_f32_16x16x32_bf16 v[124:127], v[60:63], v[36:39], v[124:127]\n\t"
      "v_mfma_f32_16x16x32_bf16 v[156:159], v[64:67], v[36:39], v[156:159]\n\t"
      "v_mfma_f32_16x16x32_bf16 v[188:191], v[68:71], v[36:39], v[188:191]\n\t"
      "ds_read_b128 v[28:31], v7 offset:39936\n\t"
      "s_waitcnt lgkmcnt(0)\n\t"
      "s_barrier\n\t"
      "v_mfma_f32_16x16x32_bf16 v[96:99], v[56:59], v[16:19], v[96:99]\n\t"
      "v_mfma_f32_16x16x32_bf16 v[128:131], v[60:63], v[16:19], v[128:131]\n\t"
      "ds_read_b128 v[40:43], v5 offset:0\n\t"
      "v_mfma_f32_16x16x32_bf16 v[160:163], v[64:67], v[16:19], v[160:163]\n\t"
      "v_mfma_f32_16x16x32_bf16 v[192:195], v[68:71], v[16:19], v[192:195]\n\t"
      "ds_read_b128 v[44:47], v5 offset:2048\n\t"
      "ds_read_b128 v[32:35], v7 offset:40960\n\t"
      "v_mfma_f32_16x16x32_bf16 v[100:103], v[56:59], v[20:23], v[100:103]\n\t"
      "v_mfma_f32_16x16x32_bf16 v[132:135], v[60:63], v[20:23], v[132:135]\n\t"
      "ds_read_b128 v[48:51], v5 offset:4096\n\t"
      "v_mfma_f32_16x16x32_bf16 v[164:167], v[64:67], v[20:23], v[164:167]\n\t"
      "v_mfma_f32_16x16x32_bf16 v[196:199], v[68:71], v[20:23], v[196:199]\n\t"
      "ds_read_b128 v[52:55], v5 offset:6144\n\t"
      "ds_read_b128 v[36:39], v7 offset:41984\n\t"
      "v_mfma_f32_16x16x32_bf16 v[200:203], v[56:59], v[24:27], v[200:203]\n\t"
      "v_mfma_f32_16x16x32_bf16 v[208:211], v[60:63], v[24:27], v[208:211]\n\t"
      "ds_read_b128 v[16:19], v7 offset:43008\n\t"
      "v_mfma_f32_16x16x32_bf16 v[204:207], v[56:59], v[28:31], v[204:207]\n\t"
      "v_mfma_f32_16x16x32_bf16 v[212:215], v[60:63], v[28:31], v[212:215]\n\t"
      "ds_read_b128 v[20:23], v7 offset:44032\n\t"
      "s_waitcnt lgkmcnt(5)\n\t"
      "v_mfma_f32_16x16x32_bf16 v[72:75], v[40:43], v[32:35], v[72:75]\n\t"
      "v_mfma_f32_16x16x32_bf16 v[104:107], v[44:47], v[32:35], v[104:107]\n\t"
      "ds_read_b128 v[56:59], v6 offset:0\n\t"
      "s_waitcnt lgkmcnt(5)\n\t"
      "v_mfma_f32_16x16x32_bf16 v[136:139], v[48:51], v[32:35], v[136:139]\n\t"
      "s_waitcnt lgkmcnt(4)\n\t"
      "v_mfma_f32_16x16x32_bf16 v[168:171], v[52:55], v[32:35], v[168:171]\n\t"
      "ds_read_b128 v[60:63], v6 offset:2048\n\t"
      "ds_read_b128 v[24:27], v7 offset:45056\n\t"
      "s_waitcnt lgkmcnt(5)\n\t"
      "v_mfma_f32_16x16x32_bf16 v[76:79], v[40:43], v[36:39], v[76:79]\n\t"
      "v_mfma_f32_16x16x32_bf16 v[108:111], v[44:47], v[36:39], v[108:111]\n\t"
      "ds_read_b128 v[64:67], v6 offset:4096\n\t"
      "v_mfma_f32_16x16x32_bf16 v[140:143], v[48:51], v[36:39], v[140:143]\n\t"
      "v_mfma_f32_16x16x32_bf16 v[172:175], v[52:55], v[36:39], v[172:175]\n\t"
      "ds_read_b128 v[68:71], v6 offset:6144\n\t"
      "ds_read_b128 v[28:31], v7 offset:46080\n\t"
      "s_waitcnt lgkmcnt(7)\n\t"
      "v_mfma_f32_16x16x32_bf16 v[80:83], v[40:43], v[16:19], v[80:83]\n\t"
      "v_mfma_f32_16x16x32_bf16 v[112:115], v[44:47], v[16:19], v[112:115]\n\t"
      "v_mfma_f32_16x16x32_bf16 v[144:147], v[48:51], v[16:19], v[144:147]\n\t"
      "v_mfma_f32_16x16x32_bf16 v[176:179], v[52:55], v[16:19], v[176:179]\n\t"
      "ds_read_b128 v[32:35], v7 offset:47104\n\t"
      "s_waitcnt lgkmcnt(7)\n\t"
      "v_mfma_f32_16x16x32_bf16 v[84:87], v[40:43], v[20:23], v[84:87]\n\t"
      "v_mfma_f32_16x16x32_bf16 v[116:119], v[44:47], v[20:23], v[116:119]\n\t"
      "v_mfma_f32_16x16x32_bf16 v[148:151], v[48:51], v[20:23], v[148:151]\n\t"
      "v_mfma_f32_16x16x32_bf16 v[180:183], v[52:55], v[20:23], v[180:183]\n\t"
      "ds_read_b128 v[36:39], v7 offset:48128\n\t"
      "s_waitcnt lgkmcnt(5)\n\t"
      "v_mfma_f32_16x16x32_bf16 v[88:91], v[40:43], v[24:27], v[88:91]\n\t"
      "v_mfma_f32_16x16x32_bf16 v[120:123], v[44:47], v[24:27], v[120:123]\n\t"
      "v_mfma_f32_16x16x32_bf16 v[152:155], v[48:51], v[24:27], v[152:155]\n\t"
      "v_mfma_f32_16x16x32_bf16 v[184:187], v[52:55], v[24:27], v[184:187]\n\t"
      "ds_read_b128 v[16:19], v7 offset:57344\n\t"
      "s_waitcnt lgkmcnt(3)\n\t"
      "v_mfma_f32_16x16x32_bf16 v[92:95], v[40:43], v[28:31], v[92:95]\n\t"
      "v_mfma_f32_16x16x32_bf16 v[124:127], v[44:47], v[28:31], v[124:127]\n\t"
      "v_mfma_f32_16x16x32_bf16 v[156:159], v[48:51], v[28:31], v[156:159]\n\t"
      "v_mfma_f32_16x16x32_bf16 v[188:191], v[52:55], v[28:31], v[188:191]\n\t"
      "ds_read_b128 v[20:23], v7 offset:58368\n\t"
      "s_waitcnt lgkmcnt(3)\n\t"
      "v_mfma_f32_16x16x32_bf16 v[96:99], v[40:43], v[32:35], v[96:99]\n\t"
      "v_mfma_f32_16x16x32_bf16 v[128:131], v[44:47], v[32:35], v[128:131]\n\t"
      "v_mfma_f32_16x16x32_bf16 v[160:163], v[48:51], v[32:35], v[160:163]\n\t"
      "v_mfma_f32_16x16x32_bf16 v[192:195], v[52:55], v[32:35], v[192:195]\n\t"
      "ds_read_b128 v[24:27], v7 offset:49152\n\t"
      "s_waitcnt lgkmcnt(3)\n\t"
      "v_mfma_f32_16x16x32_bf16 v[100:103], v[40:43], v[36:39], v[100:103]\n\t"
      "v_mfma_f32_16x16x32_bf16 v[132:135], v[44:47], v[36:39], v[132:135]\n\t"
      "v_mfma_f32_16x16x32_bf16 v[164:167], v[48:51], v[36:39], v[164:167]\n\t"
      "v_mfma_f32_16x16x32_bf16 v[196:199], v[52:55], v[36:39], v[196:199]\n\t"
      "ds_read_b128 v[28:31], v7 offset:50176\n\t"
      "s_waitcnt lgkmcnt(3)\n\t"
      "v_mfma_f32_16x16x32_bf16 v[200:203], v[40:43], v[16:19], v[200:203]\n\t"
      "v_mfma_f32_16x16x32_bf16 v[208:211], v[44:47], v[16:19], v[208:211]\n\t"
      "ds_read_b128 v[32:35], v7 offset:51200\n\t"
      "s_waitcnt lgkmcnt(3)\n\t"
      "v_mfma_f32_16x16x32_bf16 v[204:207], v[40:43], v[20:23], v[204:207]\n\t"
      "v_mfma_f32_16x16x32_bf16 v[212:215], v[44:47], v[20:23], v[212:215]\n\t"
      "ds_read_b128 v[36:39], v7 offset:52224\n\t"
      "s_waitcnt lgkmcnt(3)\n\t"
      "v_mfma_f32_16x16x32_bf16 v[72:75], v[56:59], v[24:27], v[72:75]\n\t"
      "v_mfma_f32_16x16x32_bf16 v[104:107], v[60:63], v[24:27], v[104:107]\n\t"
      "v_mfma_f32_16x16x32_bf16 v[136:139], v[64:67], v[24:27], v[136:139]\n\t"
      "v_mfma_f32_16x16x32_bf16 v[168:171], v[68:71], v[24:27], v[168:171]\n\t"
      "ds_read_b128 v[16:19], v7 offset:53248\n\t"
      "s_waitcnt lgkmcnt(3)\n\t"
      "v_mfma_f32_16x16x32_bf16 v[76:79], v[56:59], v[28:31], v[76:79]\n\t"
      "v_mfma_f32_16x16x32_bf16 v[108:111], v[60:63], v[28:31], v[108:111]\n\t"
      "v_mfma_f32_16x16x32_bf16 v[140:143], v[64:67], v[28:31], v[140:143]\n\t"
      "v_mfma_f32_16x16x32_bf16 v[172:175], v[68:71], v[28:31], v[172:175]\n\t"
      "ds_read_b128 v[20:23], v7 offset:54272\n\t"
      "s_waitcnt lgkmcnt(3)\n\t"
      "v_mfma_f32_16x16x32_bf16 v[80:83], v[56:59], v[32:35], v[80:83]\n\t"
      "v_mfma_f32_16x16x32_bf16 v[112:115], v[60:63], v[32:35], v[112:115]\n\t"
      "v_mfma_f32_16x16x32_bf16 v[144:147], v[64:67], v[32:35], v[144:147]\n\t"
      "v_mfma_f32_16x16x32_bf16 v[176:179], v[68:71], v[32:35], v[176:179]\n\t"
      "ds_read_b128 v[24:27], v7 offset:55296\n\t"
      "s_waitcnt lgkmcnt(3)\n\t"
      "v_mfma_f32_16x16x32_bf16 v[84:87], v[56:59], v[36:39], v[84:87]\n\t"
      "v_mfma_f32_16x16x32_bf16 v[116:119], v[60:63], v[36:39], v[116:119]\n\t"
      "v_mfma_f32_16x16x32_bf16 v[148:151], v[64:67], v[36:39], v[148:151]\n\t"
      "v_mfma_f32_16x16x32_bf16 v[180:183], v[68:71], v[36:39], v[180:183]\n\t"
      "ds_read_b128 v[28:31], v7 offset:56320\n\t"
      "s_waitcnt lgkmcnt(3)\n\t"
      "v_mfma_f32_16x16x32_bf16 v[88:91], v[56:59], v[16:19], v[88:91]\n\t"
      "v_mfma_f32_16x16x32_bf16 v[120:123], v[60:63], v[16:19], v[120:123]\n\t"
      "v_mfma_f32_16x16x32_bf16 v[152:155], v[64:67], v[16:19], v[152:155]\n\t"
      "v_mfma_f32_16x16x32_bf16 v[184:187], v[68:71], v[16:19], v[184:187]\n\t"
      "ds_read_b128 v[32:35], v7 offset:59392\n\t"
      "s_waitcnt lgkmcnt(3)\n\t"
      "v_mfma_f32_16x16x32_bf16 v[92:95], v[56:59], v[20:23], v[92:95]\n\t"
      "v_mfma_f32_16x16x32_bf16 v[124:127], v[60:63], v[20:23], v[124:127]\n\t"
      "v_mfma_f32_16x16x32_bf16 v[156:159], v[64:67], v[20:23], v[156:159]\n\t"
      "v_mfma_f32_16x16x32_bf16 v[188:191], v[68:71], v[20:23], v[188:191]\n\t"
      "ds_read_b128 v[36:39], v7 offset:60416\n\t"
      "s_waitcnt lgkmcnt(0)\n\t"
      "s_barrier\n\t"
      "v_mfma_f32_16x16x32_bf16 v[96:99], v[56:59], v[24:27], v[96:99]\n\t"
      "v_mfma_f32_16x16x32_bf16 v[128:131], v[60:63], v[24:27], v[128:131]\n\t"
      "ds_read_b128 v[40:43], v3 offset:0\n\t"
      "v_mfma_f32_16x16x32_bf16 v[160:163], v[64:67], v[24:27], v[160:163]\n\t"
      "v_mfma_f32_16x16x32_bf16 v[192:195], v[68:71], v[24:27], v[192:195]\n\t"
      "ds_read_b128 v[44:47], v3 offset:2048\n\t"
      "ds_read_b128 v[16:19], v7 offset:0\n\t"
      "v_mfma_f32_16x16x32_bf16 v[100:103], v[56:59], v[28:31], v[100:103]\n\t"
      "v_mfma_f32_16x16x32_bf16 v[132:135], v[60:63], v[28:31], v[132:135]\n\t"
      "ds_read_b128 v[48:51], v3 offset:4096\n\t"
      "v_mfma_f32_16x16x32_bf16 v[164:167], v[64:67], v[28:31], v[164:167]\n\t"
      "v_mfma_f32_16x16x32_bf16 v[196:199], v[68:71], v[28:31], v[196:199]\n\t"
      "ds_read_b128 v[52:55], v3 offset:6144\n\t"
      "ds_read_b128 v[20:23], v7 offset:1024\n\t"
      "v_mfma_f32_16x16x32_bf16 v[200:203], v[56:59], v[32:35], v[200:203]\n\t"
      "v_mfma_f32_16x16x32_bf16 v[208:211], v[60:63], v[32:35], v[208:211]\n\t"
      "ds_read_b128 v[24:27], v7 offset:2048\n\t"
      "v_mfma_f32_16x16x32_bf16 v[204:207], v[56:59], v[36:39], v[204:207]\n\t"
      "v_mfma_f32_16x16x32_bf16 v[212:215], v[60:63], v[36:39], v[212:215]\n\t"
      "ds_read_b128 v[28:31], v7 offset:3072\n\t"
      "s_waitcnt lgkmcnt(5)\n\t"
      "v_mfma_f32_16x16x32_bf16 v[72:75], v[40:43], v[16:19], v[72:75]\n\t"
      "v_mfma_f32_16x16x32_bf16 v[104:107], v[44:47], v[16:19], v[104:107]\n\t"
      "ds_read_b128 v[56:59], v4 offset:0\n\t"
      "s_waitcnt lgkmcnt(5)\n\t"
      "v_mfma_f32_16x16x32_bf16 v[136:139], v[48:51], v[16:19], v[136:139]\n\t"
      "s_waitcnt lgkmcnt(4)\n\t"
      "v_mfma_f32_16x16x32_bf16 v[168:171], v[52:55], v[16:19], v[168:171]\n\t"
      "ds_read_b128 v[60:63], v4 offset:2048\n\t"
      "ds_read_b128 v[32:35], v7 offset:8192\n\t"
      "s_waitcnt lgkmcnt(5)\n\t"
      "v_mfma_f32_16x16x32_bf16 v[76:79], v[40:43], v[20:23], v[76:79]\n\t"
      "v_mfma_f32_16x16x32_bf16 v[108:111], v[44:47], v[20:23], v[108:111]\n\t"
      "ds_read_b128 v[64:67], v4 offset:4096\n\t"
      "v_mfma_f32_16x16x32_bf16 v[140:143], v[48:51], v[20:23], v[140:143]\n\t"
      "v_mfma_f32_16x16x32_bf16 v[172:175], v[52:55], v[20:23], v[172:175]\n\t"
      "ds_read_b128 v[68:71], v4 offset:6144\n\t"
      "ds_read_b128 v[36:39], v7 offset:9216\n\t"
      "s_waitcnt lgkmcnt(7)\n\t"
      "v_mfma_f32_16x16x32_bf16 v[80:83], v[40:43], v[24:27], v[80:83]\n\t"
      "v_mfma_f32_16x16x32_bf16 v[112:115], v[44:47], v[24:27], v[112:115]\n\t"
      "v_mfma_f32_16x16x32_bf16 v[144:147], v[48:51], v[24:27], v[144:147]\n\t"
      "v_mfma_f32_16x16x32_bf16 v[176:179], v[52:55], v[24:27], v[176:179]\n\t"
      "ds_read_b128 v[16:19], v7 offset:10240\n\t"
      "s_waitcnt lgkmcnt(7)\n\t"
      "v_mfma_f32_16x16x32_bf16 v[84:87], v[40:43], v[28:31], v[84:87]\n\t"
      "v_mfma_f32_16x16x32_bf16 v[116:119], v[44:47], v[28:31], v[116:119]\n\t"
      "v_mfma_f32_16x16x32_bf16 v[148:151], v[48:51], v[28:31], v[148:151]\n\t"
      "v_mfma_f32_16x16x32_bf16 v[180:183], v[52:55], v[28:31], v[180:183]\n\t"
      "ds_read_b128 v[20:23], v7 offset:11264\n\t"
      "s_waitcnt lgkmcnt(5)\n\t"
      "v_mfma_f32_16x16x32_bf16 v[72:75], v[56:59], v[32:35], v[72:75]\n\t"
      "v_mfma_f32_16x16x32_bf16 v[104:107], v[60:63], v[32:35], v[104:107]\n\t"
      "ds_read_b128 v[40:43], v3 offset:32768\n\t"
      "s_waitcnt lgkmcnt(5)\n\t"
      "v_mfma_f32_16x16x32_bf16 v[136:139], v[64:67], v[32:35], v[136:139]\n\t"
      "s_waitcnt lgkmcnt(4)\n\t"
      "v_mfma_f32_16x16x32_bf16 v[168:171], v[68:71], v[32:35], v[168:171]\n\t"
      "ds_read_b128 v[44:47], v3 offset:34816\n\t"
      "ds_read_b128 v[24:27], v7 offset:20480\n\t"
      "s_waitcnt lgkmcnt(5)\n\t"
      "v_mfma_f32_16x16x32_bf16 v[76:79], v[56:59], v[36:39], v[76:79]\n\t"
      "v_mfma_f32_16x16x32_bf16 v[108:111], v[60:63], v[36:39], v[108:111]\n\t"
      "ds_read_b128 v[48:51], v3 offset:36864\n\t"
      "v_mfma_f32_16x16x32_bf16 v[140:143], v[64:67], v[36:39], v[140:143]\n\t"
      "v_mfma_f32_16x16x32_bf16 v[172:175], v[68:71], v[36:39], v[172:175]\n\t"
      "ds_read_b128 v[52:55], v3 offset:38912\n\t"
      "ds_read_b128 v[28:31], v7 offset:21504\n\t"
      "s_waitcnt lgkmcnt(7)\n\t"
      "v_mfma_f32_16x16x32_bf16 v[80:83], v[56:59], v[16:19], v[80:83]\n\t"
      "v_mfma_f32_16x16x32_bf16 v[112:115], v[60:63], v[16:19], v[112:115]\n\t"
      "v_mfma_f32_16x16x32_bf16 v[144:147], v[64:67], v[16:19], v[144:147]\n\t"
      "v_mfma_f32_16x16x32_bf16 v[176:179], v[68:71], v[16:19], v[176:179]\n\t"
      "ds_read_b128 v[32:35], v7 offset:22528\n\t"
      "s_waitcnt lgkmcnt(7)\n\t"
      "v_mfma_f32_16x16x32_bf16 v[84:87], v[56:59], v[20:23], v[84:87]\n\t"
      "v_mfma_f32_16x16x32_bf16 v[116:119], v[60:63], v[20:23], v[116:119]\n\t"
      "v_mfma_f32_16x16x32_bf16 v[148:151], v[64:67], v[20:23], v[148:151]\n\t"
      "v_mfma_f32_16x16x32_bf16 v[180:183], v[68:71], v[20:23], v[180:183]\n\t"
      "ds_read_b128 v[36:39], v7 offset:23552\n\t"
      "s_waitcnt lgkmcnt(5)\n\t"
      "v_mfma_f32_16x16x32_bf16 v[72:75], v[40:43], v[24:27], v[72:75]\n\t"
      "v_mfma_f32_16x16x32_bf16 v[104:107], v[44:47], v[24:27], v[104:107]\n\t"
      "ds_read_b128 v[56:59], v4 offset:32768\n\t"
      "s_waitcnt lgkmcnt(5)\n\t"
      "v_mfma_f32_16x16x32_bf16 v[136:139], v[48:51], v[24:27], v[136:139]\n\t"
      "s_waitcnt lgkmcnt(4)\n\t"
      "v_mfma_f32_16x16x32_bf16 v[168:171], v[52:55], v[24:27], v[168:171]\n\t"
      "ds_read_b128 v[60:63], v4 offset:34816\n\t"
      "ds_read_b128 v[16:19], v7 offset:28672\n\t"
      "s_waitcnt lgkmcnt(5)\n\t"
      "v_mfma_f32_16x16x32_bf16 v[76:79], v[40:43], v[28:31], v[76:79]\n\t"
      "v_mfma_f32_16x16x32_bf16 v[108:111], v[44:47], v[28:31], v[108:111]\n\t"
      "ds_read_b128 v[64:67], v4 offset:36864\n\t"
      "v_mfma_f32_16x16x32_bf16 v[140:143], v[48:51], v[28:31], v[140:143]\n\t"
      "v_mfma_f32_16x16x32_bf16 v[172:175], v[52:55], v[28:31], v[172:175]\n\t"
      "ds_read_b128 v[68:71], v4 offset:38912\n\t"
      "ds_read_b128 v[20:23], v7 offset:29696\n\t"
      "s_waitcnt lgkmcnt(7)\n\t"
      "v_mfma_f32_16x16x32_bf16 v[80:83], v[40:43], v[32:35], v[80:83]\n\t"
      "v_mfma_f32_16x16x32_bf16 v[112:115], v[44:47], v[32:35], v[112:115]\n\t"
      "v_mfma_f32_16x16x32_bf16 v[144:147], v[48:51], v[32:35], v[144:147]\n\t"
      "v_mfma_f32_16x16x32_bf16 v[176:179], v[52:55], v[32:35], v[176:179]\n\t"
      "ds_read_b128 v[24:27], v7 offset:30720\n\t"
      "s_waitcnt lgkmcnt(7)\n\t"
      "v_mfma_f32_16x16x32_bf16 v[84:87], v[40:43], v[36:39], v[84:87]\n\t"
      "v_mfma_f32_16x16x32_bf16 v[116:119], v[44:47], v[36:39], v[116:119]\n\t"
      "v_mfma_f32_16x16x32_bf16 v[148:151], v[48:51], v[36:39], v[148:151]\n\t"
      "v_mfma_f32_16x16x32_bf16 v[180:183], v[52:55], v[36:39], v[180:183]\n\t"
      "ds_read_b128 v[28:31], v7 offset:31744\n\t"
      "s_waitcnt lgkmcnt(5)\n\t"
      "v_mfma_f32_16x16x32_bf16 v[72:75], v[56:59], v[16:19], v[72:75]\n\t"
      "v_mfma_f32_16x16x32_bf16 v[104:107], v[60:63], v[16:19], v[104:107]\n\t"
      "s_waitcnt lgkmcnt(4)\n\t"
      "v_mfma_f32_16x16x32_bf16 v[136:139], v[64:67], v[16:19], v[136:139]\n\t"
      "s_waitcnt lgkmcnt(3)\n\t"
      "v_mfma_f32_16x16x32_bf16 v[168:171], v[68:71], v[16:19], v[168:171]\n\t"
      "s_waitcnt lgkmcnt(2)\n\t"
      "v_mfma_f32_16x16x32_bf16 v[76:79], v[56:59], v[20:23], v[76:79]\n\t"
      "v_mfma_f32_16x16x32_bf16 v[108:111], v[60:63], v[20:23], v[108:111]\n\t"
      "v_mfma_f32_16x16x32_bf16 v[140:143], v[64:67], v[20:23], v[140:143]\n\t"
      "v_mfma_f32_16x16x32_bf16 v[172:175], v[68:71], v[20:23], v[172:175]\n\t"
      "s_barrier\n\t"
      "ds_read_b128 v[32:35], v7 offset:40960\n\t"
      "ds_read_b128 v[36:39], v7 offset:41984\n\t"
      "s_waitcnt lgkmcnt(3)\n\t"
      "v_mfma_f32_16x16x32_bf16 v[80:83], v[56:59], v[24:27], v[80:83]\n\t"
      "v_mfma_f32_16x16x32_bf16 v[112:115], v[60:63], v[24:27], v[112:115]\n\t"
      "ds_read_b128 v[40:43], v5 offset:0\n\t"
      "v_mfma_f32_16x16x32_bf16 v[144:147], v[64:67], v[24:27], v[144:147]\n\t"
      "v_mfma_f32_16x16x32_bf16 v[176:179], v[68:71], v[24:27], v[176:179]\n\t"
      "ds_read_b128 v[44:47], v5 offset:2048\n\t"
      "ds_read_b128 v[16:19], v7 offset:43008\n\t"
      "s_waitcnt lgkmcnt(5)\n\t"
      "v_mfma_f32_16x16x32_bf16 v[84:87], v[56:59], v[28:31], v[84:87]\n\t"
      "v_mfma_f32_16x16x32_bf16 v[116:119], v[60:63], v[28:31], v[116:119]\n\t"
      "ds_read_b128 v[48:51], v5 offset:4096\n\t"
      "v_mfma_f32_16x16x32_bf16 v[148:151], v[64:67], v[28:31], v[148:151]\n\t"
      "v_mfma_f32_16x16x32_bf16 v[180:183], v[68:71], v[28:31], v[180:183]\n\t"
      "ds_read_b128 v[52:55], v5 offset:6144\n\t"
      "ds_read_b128 v[20:23], v7 offset:44032\n\t"
      "s_waitcnt lgkmcnt(5)\n\t"
      "v_mfma_f32_16x16x32_bf16 v[72:75], v[40:43], v[32:35], v[72:75]\n\t"
      "s_waitcnt lgkmcnt(4)\n\t"
      "v_mfma_f32_16x16x32_bf16 v[104:107], v[44:47], v[32:35], v[104:107]\n\t"
      "ds_read_b128 v[56:59], v6 offset:0\n\t"
      "s_waitcnt lgkmcnt(3)\n\t"
      "v_mfma_f32_16x16x32_bf16 v[136:139], v[48:51], v[32:35], v[136:139]\n\t"
      "s_waitcnt lgkmcnt(2)\n\t"
      "v_mfma_f32_16x16x32_bf16 v[168:171], v[52:55], v[32:35], v[168:171]\n\t"
      "ds_read_b128 v[60:63], v6 offset:2048\n\t"
      "ds_read_b128 v[24:27], v7 offset:49152\n\t"
      "v_mfma_f32_16x16x32_bf16 v[76:79], v[40:43], v[36:39], v[76:79]\n\t"
      "v_mfma_f32_16x16x32_bf16 v[108:111], v[44:47], v[36:39], v[108:111]\n\t"
      "ds_read_b128 v[64:67], v6 offset:4096\n\t"
      "v_mfma_f32_16x16x32_bf16 v[140:143], v[48:51], v[36:39], v[140:143]\n\t"
      "v_mfma_f32_16x16x32_bf16 v[172:175], v[52:55], v[36:39], v[172:175]\n\t"
      "ds_read_b128 v[68:71], v6 offset:6144\n\t"
      "ds_read_b128 v[28:31], v7 offset:50176\n\t"
      "v_mfma_f32_16x16x32_bf16 v[80:83], v[40:43], v[16:19], v[80:83]\n\t"
      "v_mfma_f32_16x16x32_bf16 v[112:115], v[44:47], v[16:19], v[112:115]\n\t"
      "v_mfma_f32_16x16x32_bf16 v[144:147], v[48:51], v[16:19], v[144:147]\n\t"
      "v_mfma_f32_16x16x32_bf16 v[176:179], v[52:55], v[16:19], v[176:179]\n\t"
      "ds_read_b128 v[32:35], v7 offset:51200\n\t"
      "s_waitcnt lgkmcnt(7)\n\t"
      "v_mfma_f32_16x16x32_bf16 v[84:87], v[40:43], v[20:23], v[84:87]\n\t"
      "v_mfma_f32_16x16x32_bf16 v[116:119], v[44:47], v[20:23], v[116:119]\n\t"
      "v_mfma_f32_16x16x32_bf16 v[148:151], v[48:51], v[20:23], v[148:151]\n\t"
      "v_mfma_f32_16x16x32_bf16 v[180:183], v[52:55], v[20:23], v[180:183]\n\t"
      "ds_read_b128 v[36:39], v7 offset:52224\n\t"
      "s_waitcnt lgkmcnt(5)\n\t"
      "v_mfma_f32_16x16x32_bf16 v[72:75], v[56:59], v[24:27], v[72:75]\n\t"
      "v_mfma_f32_16x16x32_bf16 v[104:107], v[60:63], v[24:27], v[104:107]\n\t"
      "ds_read_b128 v[40:43], v3 offset:0\n\t"
      "s_waitcnt lgkmcnt(5)\n\t"
      "v_mfma_f32_16x16x32_bf16 v[136:139], v[64:67], v[24:27], v[136:139]\n\t"
      "s_waitcnt lgkmcnt(4)\n\t"
      "v_mfma_f32_16x16x32_bf16 v[168:171], v[68:71], v[24:27], v[168:171]\n\t"
      "ds_read_b128 v[44:47], v3 offset:2048\n\t"
      "ds_read_b128 v[16:19], v7 offset:4096\n\t"
      "s_waitcnt lgkmcnt(5)\n\t"
      "v_mfma_f32_16x16x32_bf16 v[76:79], v[56:59], v[28:31], v[76:79]\n\t"
      "v_mfma_f32_16x16x32_bf16 v[108:111], v[60:63], v[28:31], v[108:111]\n\t"
      "ds_read_b128 v[48:51], v3 offset:4096\n\t"
      "v_mfma_f32_16x16x32_bf16 v[140:143], v[64:67], v[28:31], v[140:143]\n\t"
      "v_mfma_f32_16x16x32_bf16 v[172:175], v[68:71], v[28:31], v[172:175]\n\t"
      "ds_read_b128 v[52:55], v3 offset:6144\n\t"
      "ds_read_b128 v[20:23], v7 offset:5120\n\t"
      "s_waitcnt lgkmcnt(7)\n\t"
      "v_mfma_f32_16x16x32_bf16 v[80:83], v[56:59], v[32:35], v[80:83]\n\t"
      "v_mfma_f32_16x16x32_bf16 v[112:115], v[60:63], v[32:35], v[112:115]\n\t"
      "v_mfma_f32_16x16x32_bf16 v[144:147], v[64:67], v[32:35], v[144:147]\n\t"
      "v_mfma_f32_16x16x32_bf16 v[176:179], v[68:71], v[32:35], v[176:179]\n\t"
      "ds_read_b128 v[24:27], v7 offset:6144\n\t"
      "s_waitcnt lgkmcnt(7)\n\t"
      "v_mfma_f32_16x16x32_bf16 v[84:87], v[56:59], v[36:39], v[84:87]\n\t"
      "v_mfma_f32_16x16x32_bf16 v[116:119], v[60:63], v[36:39], v[116:119]\n\t"
      "v_mfma_f32_16x16x32_bf16 v[148:151], v[64:67], v[36:39], v[148:151]\n\t"
      "v_mfma_f32_16x16x32_bf16 v[180:183], v[68:71], v[36:39], v[180:183]\n\t"
      "ds_read_b128 v[28:31], v7 offset:7168\n\t"
      "s_waitcnt lgkmcnt(5)\n\t"
      "v_mfma_f32_16x16x32_bf16 v[88:91], v[40:43], v[16:19], v[88:91]\n\t"
      "v_cvt_pk_bf16_f32 v72, v72, v76\n\t"
      "v_mfma_f32_16x16x32_bf16 v[120:123], v[44:47], v[16:19], v[120:123]\n\t"
      "ds_read_b128 v[56:59], v4 offset:0\n\t"
      "v_cvt_pk_bf16_f32 v76, v73, v77\n\t"
      "s_waitcnt lgkmcnt(5)\n\t"
      "v_mfma_f32_16x16x32_bf16 v[152:155], v[48:51], v[16:19], v[152:155]\n\t"
      "v_cvt_pk_bf16_f32 v73, v80, v84\n\t"
      "s_waitcnt lgkmcnt(4)\n\t"
      "v_mfma_f32_16x16x32_bf16 v[184:187], v[52:55], v[16:19], v[184:187]\n\t"
      "ds_read_b128 v[60:63], v4 offset:2048\n\t"
      "v_cvt_pk_bf16_f32 v77, v81, v85\n\t"
      "ds_read_b128 v[32:35], v7 offset:12288\n\t"
      "s_waitcnt lgkmcnt(5)\n\t"
      "v_mfma_f32_16x16x32_bf16 v[92:95], v[40:43], v[20:23], v[92:95]\n\t"
      "buffer_store_dwordx2 v[72:73], v8, s[36:39], s60 offen sc1\n\t"
      "v_mfma_f32_16x16x32_bf16 v[124:127], v[44:47], v[20:23], v[124:127]\n\t"
      "ds_read_b128 v[64:67], v4 offset:4096\n\t"
      "buffer_store_dwordx2 v[76:77], v8, s[36:39], s61 offen sc1\n\t"
      "v_mfma_f32_16x16x32_bf16 v[156:159], v[48:51], v[20:23], v[156:159]\n\t"
      "v_cvt_pk_bf16_f32 v74, v74, v78\n\t"
      "v_mfma_f32_16x16x32_bf16 v[188:191], v[52:55], v[20:23], v[188:191]\n\t"
      "ds_read_b128 v[68:71], v4 offset:6144\n\t"
      "v_cvt_pk_bf16_f32 v78, v75, v79\n\t"
      "ds_read_b128 v[36:39], v7 offset:13312\n\t"
      "s_waitcnt lgkmcnt(7)\n\t"
      "v_mfma_f32_16x16x32_bf16 v[96:99], v[40:43], v[24:27], v[96:99]\n\t"
      "v_cvt_pk_bf16_f32 v75, v82, v86\n\t"
      "v_mfma_f32_16x16x32_bf16 v[128:131], v[44:47], v[24:27], v[128:131]\n\t"
      "v_cvt_pk_bf16_f32 v79, v83, v87\n\t"
      "v_mfma_f32_16x16x32_bf16 v[160:163], v[48:51], v[24:27], v[160:163]\n\t"
      "buffer_store_dwordx2 v[74:75], v8, s[36:39], s62 offen sc1\n\t"
      "v_mfma_f32_16x16x32_bf16 v[192:195], v[52:55], v[24:27], v[192:195]\n\t"
      "buffer_store_dwordx2 v[78:79], v8, s[36:39], s63 offen sc1\n\t"
      "ds_read_b128 v[16:19], v7 offset:14336\n\t"
      "s_waitcnt lgkmcnt(7)\n\t"
      "v_mfma_f32_16x16x32_bf16 v[100:103], v[40:43], v[28:31], v[100:103]\n\t"
      "v_cvt_pk_bf16_f32 v104, v104, v108\n\t"
      "v_mfma_f32_16x16x32_bf16 v[132:135], v[44:47], v[28:31], v[132:135]\n\t"
      "v_cvt_pk_bf16_f32 v108, v105, v109\n\t"
      "v_mfma_f32_16x16x32_bf16 v[164:167], v[48:51], v[28:31], v[164:167]\n\t"
      "v_cvt_pk_bf16_f32 v105, v112, v116\n\t"
      "v_mfma_f32_16x16x32_bf16 v[196:199], v[52:55], v[28:31], v[196:199]\n\t"
      "v_cvt_pk_bf16_f32 v109, v113, v117\n\t"
      "ds_read_b128 v[20:23], v7 offset:15360\n\t"
      "s_waitcnt lgkmcnt(5)\n\t"
      "v_mfma_f32_16x16x32_bf16 v[88:91], v[56:59], v[32:35], v[88:91]\n\t"
      "buffer_store_dwordx2 v[104:105], v8, s[36:39], s64 offen sc1\n\t"
      "v_mfma_f32_16x16x32_bf16 v[120:123], v[60:63], v[32:35], v[120:123]\n\t"
      "buffer_store_dwordx2 v[108:109], v8, s[36:39], s65 offen sc1\n\t"
      "s_waitcnt lgkmcnt(4)\n\t"
      "v_mfma_f32_16x16x32_bf16 v[152:155], v[64:67], v[32:35], v[152:155]\n\t"
      "v_cvt_pk_bf16_f32 v106, v106, v110\n\t"
      "s_waitcnt lgkmcnt(3)\n\t"
      "v_mfma_f32_16x16x32_bf16 v[184:187], v[68:71], v[32:35], v[184:187]\n\t"
      "v_cvt_pk_bf16_f32 v110, v107, v111\n\t"
      "s_waitcnt lgkmcnt(2)\n\t"
      "v_mfma_f32_16x16x32_bf16 v[92:95], v[56:59], v[36:39], v[92:95]\n\t"
      "v_cvt_pk_bf16_f32 v107, v114, v118\n\t"
      "v_mfma_f32_16x16x32_bf16 v[124:127], v[60:63], v[36:39], v[124:127]\n\t"
      "v_cvt_pk_bf16_f32 v111, v115, v119\n\t"
      "v_mfma_f32_16x16x32_bf16 v[156:159], v[64:67], v[36:39], v[156:159]\n\t"
      "buffer_store_dwordx2 v[106:107], v8, s[36:39], s66 offen sc1\n\t"
      "v_mfma_f32_16x16x32_bf16 v[188:191], v[68:71], v[36:39], v[188:191]\n\t"
      "buffer_store_dwordx2 v[110:111], v8, s[36:39], s67 offen sc1\n\t"
      "s_barrier\n\t"
      "ds_read_b128 v[24:27], v7 offset:24576\n\t"
      "ds_read_b128 v[28:31], v7 offset:25600\n\t"
      "s_waitcnt lgkmcnt(3)\n\t"
      "v_mfma_f32_16x16x32_bf16 v[96:99], v[56:59], v[16:19], v[96:99]\n\t"
      "v_cvt_pk_bf16_f32 v136, v136, v140\n\t"
      "v_mfma_f32_16x16x32_bf16 v[128:131], v[60:63], v[16:19], v[128:131]\n\t"
      "ds_read_b128 v[40:43], v3 offset:32768\n\t"
      "v_cvt_pk_bf16_f32 v140, v137, v141\n\t"
      "v_mfma_f32_16x16x32_bf16 v[160:163], v[64:67], v[16:19], v[160:163]\n\t"
      "v_cvt_pk_bf16_f32 v137, v144, v148\n\t"
      "v_mfma_f32_16x16x32_bf16 v[192:195], v[68:71], v[16:19], v[192:195]\n\t"
      "ds_read_b128 v[44:47], v3 offset:34816\n\t"
      "v_cvt_pk_bf16_f32 v141, v145, v149\n\t"
      "ds_read_b128 v[32:35], v7 offset:26624\n\t"
      "s_waitcnt lgkmcnt(5)\n\t"
      "v_mfma_f32_16x16x32_bf16 v[100:103], v[56:59], v[20:23], v[100:103]\n\t"
      "buffer_store_dwordx2 v[136:137], v9, s[36:39], s60 offen sc1\n\t"
      "v_mfma_f32_16x16x32_bf16 v[132:135], v[60:63], v[20:23], v[132:135]\n\t"
      "ds_read_b128 v[48:51], v3 offset:36864\n\t"
      "buffer_store_dwordx2 v[140:141], v9, s[36:39], s61 offen sc1\n\t"
      "v_mfma_f32_16x16x32_bf16 v[164:167], v[64:67], v[20:23], v[164:167]\n\t"
      "v_cvt_pk_bf16_f32 v138, v138, v142\n\t"
      "v_mfma_f32_16x16x32_bf16 v[196:199], v[68:71], v[20:23], v[196:199]\n\t"
      "ds_read_b128 v[52:55], v3 offset:38912\n\t"
      "v_cvt_pk_bf16_f32 v142, v139, v143\n\t"
      "ds_read_b128 v[36:39], v7 offset:27648\n\t"
      "s_waitcnt lgkmcnt(5)\n\t"
      "v_mfma_f32_16x16x32_bf16 v[88:91], v[40:43], v[24:27], v[88:91]\n\t"
      "v_cvt_pk_bf16_f32 v139, v146, v150\n\t"
      "s_waitcnt lgkmcnt(4)\n\t"
      "v_mfma_f32_16x16x32_bf16 v[120:123], v[44:47], v[24:27], v[120:123]\n\t"
      "ds_read_b128 v[56:59], v4 offset:32768\n\t"
      "v_cvt_pk_bf16_f32 v143, v147, v151\n\t"
      "s_waitcnt lgkmcnt(3)\n\t"
      "v_mfma_f32_16x16x32_bf16 v[152:155], v[48:51], v[24:27], v[152:155]\n\t"
      "buffer_store_dwordx2 v[138:139], v9, s[36:39], s62 offen sc1\n\t"
      "s_waitcnt lgkmcnt(2)\n\t"
      "v_mfma_f32_16x16x32_bf16 v[184:187], v[52:55], v[24:27], v[184:187]\n\t"
      "ds_read_b128 v[60:63], v4 offset:34816\n\t"
      "buffer_store_dwordx2 v[142:143], v9, s[36:39], s63 offen sc1\n\t"
      "ds_read_b128 v[16:19], v7 offset:32768\n\t"
      "v_mfma_f32_16x16x32_bf16 v[92:95], v[40:43], v[28:31], v[92:95]\n\t"
      "v_cvt_pk_bf16_f32 v168, v168, v172\n\t"
      "v_mfma_f32_16x16x32_bf16 v[124:127], v[44:47], v[28:31], v[124:127]\n\t"
      "ds_read_b128 v[64:67], v4 offset:36864\n\t"
      "v_cvt_pk_bf16_f32 v172, v169, v173\n\t"
      "v_mfma_f32_16x16x32_bf16 v[156:159], v[48:51], v[28:31], v[156:159]\n\t"
      "v_cvt_pk_bf16_f32 v169, v176, v180\n\t"
      "v_mfma_f32_16x16x32_bf16 v[188:191], v[52:55], v[28:31], v[188:191]\n\t"
      "ds_read_b128 v[68:71], v4 offset:38912\n\t"
      "v_cvt_pk_bf16_f32 v173, v177, v181\n\t"
      "ds_read_b128 v[20:23], v7 offset:33792\n\t"
      "v_mfma_f32_16x16x32_bf16 v[96:99], v[40:43], v[32:35], v[96:99]\n\t"
      "buffer_store_dwordx2 v[168:169], v9, s[36:39], s64 offen sc1\n\t"
      "v_mfma_f32_16x16x32_bf16 v[128:131], v[44:47], v[32:35], v[128:131]\n\t"
      "buffer_store_dwordx2 v[172:173], v9, s[36:39], s65 offen sc1\n\t"
      "v_mfma_f32_16x16x32_bf16 v[160:163], v[48:51], v[32:35], v[160:163]\n\t"
      "v_cvt_pk_bf16_f32 v170, v170, v174\n\t"
      "v_mfma_f32_16x16x32_bf16 v[192:195], v[52:55], v[32:35], v[192:195]\n\t"
      "v_cvt_pk_bf16_f32 v174, v171, v175\n\t"
      "ds_read_b128 v[24:27], v7 offset:34816\n\t"
      "s_waitcnt lgkmcnt(7)\n\t"
      "v_mfma_f32_16x16x32_bf16 v[100:103], v[40:43], v[36:39], v[100:103]\n\t"
      "v_cvt_pk_bf16_f32 v171, v178, v182\n\t"
      "v_mfma_f32_16x16x32_bf16 v[132:135], v[44:47], v[36:39], v[132:135]\n\t"
      "v_cvt_pk_bf16_f32 v175, v179, v183\n\t"
      "v_mfma_f32_16x16x32_bf16 v[164:167], v[48:51], v[36:39], v[164:167]\n\t"
      "buffer_store_dwordx2 v[170:171], v9, s[36:39], s66 offen sc1\n\t"
      "v_mfma_f32_16x16x32_bf16 v[196:199], v[52:55], v[36:39], v[196:199]\n\t"
      "buffer_store_dwordx2 v[174:175], v9, s[36:39], s67 offen sc1\n\t"
      "ds_read_b128 v[28:31], v7 offset:35840\n\t"
      "s_waitcnt lgkmcnt(5)\n\t"
      "v_mfma_f32_16x16x32_bf16 v[88:91], v[56:59], v[16:19], v[88:91]\n\t"
      "v_mfma_f32_16x16x32_bf16 v[120:123], v[60:63], v[16:19], v[120:123]\n\t"
      "ds_read_b128 v[40:43], v5 offset:0\n\t"
      "s_waitcnt lgkmcnt(5)\n\t"
      "v_mfma_f32_16x16x32_bf16 v[152:155], v[64:67], v[16:19], v[152:155]\n\t"
      "s_waitcnt lgkmcnt(4)\n\t"
      "v_mfma_f32_16x16x32_bf16 v[184:187], v[68:71], v[16:19], v[184:187]\n\t"
      "ds_read_b128 v[44:47], v5 offset:2048\n\t"
      "ds_read_b128 v[32:35], v7 offset:45056\n\t"
      "s_waitcnt lgkmcnt(5)\n\t"
      "v_mfma_f32_16x16x32_bf16 v[92:95], v[56:59], v[20:23], v[92:95]\n\t"
      "v_mfma_f32_16x16x32_bf16 v[124:127], v[60:63], v[20:23], v[124:127]\n\t"
      "ds_read_b128 v[48:51], v5 offset:4096\n\t"
      "v_mfma_f32_16x16x32_bf16 v[156:159], v[64:67], v[20:23], v[156:159]\n\t"
      "v_mfma_f32_16x16x32_bf16 v[188:191], v[68:71], v[20:23], v[188:191]\n\t"
      "ds_read_b128 v[52:55], v5 offset:6144\n\t"
      "ds_read_b128 v[36:39], v7 offset:46080\n\t"
      "s_waitcnt lgkmcnt(7)\n\t"
      "v_mfma_f32_16x16x32_bf16 v[96:99], v[56:59], v[24:27], v[96:99]\n\t"
      "v_mfma_f32_16x16x32_bf16 v[128:131], v[60:63], v[24:27], v[128:131]\n\t"
      "v_mfma_f32_16x16x32_bf16 v[160:163], v[64:67], v[24:27], v[160:163]\n\t"
      "v_mfma_f32_16x16x32_bf16 v[192:195], v[68:71], v[24:27], v[192:195]\n\t"
      "ds_read_b128 v[16:19], v7 offset:47104\n\t"
      "s_waitcnt lgkmcnt(7)\n\t"
      "v_mfma_f32_16x16x32_bf16 v[100:103], v[56:59], v[28:31], v[100:103]\n\t"
      "v_mfma_f32_16x16x32_bf16 v[132:135], v[60:63], v[28:31], v[132:135]\n\t"
      "v_mfma_f32_16x16x32_bf16 v[164:167], v[64:67], v[28:31], v[164:167]\n\t"
      "v_mfma_f32_16x16x32_bf16 v[196:199], v[68:71], v[28:31], v[196:199]\n\t"
      "ds_read_b128 v[20:23], v7 offset:48128\n\t"
      "s_barrier\n\t"
      "s_waitcnt lgkmcnt(5)\n\t"
      "v_mfma_f32_16x16x32_bf16 v[88:91], v[40:43], v[32:35], v[88:91]\n\t"
      "v_mfma_f32_16x16x32_bf16 v[120:123], v[44:47], v[32:35], v[120:123]\n\t"
      "ds_read_b128 v[56:59], v6 offset:0\n\t"
      "s_waitcnt lgkmcnt(5)\n\t"
      "v_mfma_f32_16x16x32_bf16 v[152:155], v[48:51], v[32:35], v[152:155]\n\t"
      "s_waitcnt lgkmcnt(4)\n\t"
      "v_mfma_f32_16x16x32_bf16 v[184:187], v[52:55], v[32:35], v[184:187]\n\t"
      "ds_read_b128 v[60:63], v6 offset:2048\n\t"
      "ds_read_b128 v[24:27], v7 offset:53248\n\t"
      "s_waitcnt lgkmcnt(5)\n\t"
      "v_mfma_f32_16x16x32_bf16 v[92:95], v[40:43], v[36:39], v[92:95]\n\t"
      "v_mfma_f32_16x16x32_bf16 v[124:127], v[44:47], v[36:39], v[124:127]\n\t"
      "ds_read_b128 v[64:67], v6 offset:4096\n\t"
      "v_mfma_f32_16x16x32_bf16 v[156:159], v[48:51], v[36:39], v[156:159]\n\t"
      "v_mfma_f32_16x16x32_bf16 v[188:191], v[52:55], v[36:39], v[188:191]\n\t"
      "ds_read_b128 v[68:71], v6 offset:6144\n\t"
      "ds_read_b128 v[28:31], v7 offset:54272\n\t"
      "s_waitcnt lgkmcnt(7)\n\t"
      "v_mfma_f32_16x16x32_bf16 v[96:99], v[40:43], v[16:19], v[96:99]\n\t"
      "v_mfma_f32_16x16x32_bf16 v[128:131], v[44:47], v[16:19], v[128:131]\n\t"
      "v_mfma_f32_16x16x32_bf16 v[160:163], v[48:51], v[16:19], v[160:163]\n\t"
      "v_mfma_f32_16x16x32_bf16 v[192:195], v[52:55], v[16:19], v[192:195]\n\t"
      "ds_read_b128 v[32:35], v7 offset:55296\n\t"
      "s_waitcnt lgkmcnt(7)\n\t"
      "v_mfma_f32_16x16x32_bf16 v[100:103], v[40:43], v[20:23], v[100:103]\n\t"
      "v_mfma_f32_16x16x32_bf16 v[132:135], v[44:47], v[20:23], v[132:135]\n\t"
      "v_mfma_f32_16x16x32_bf16 v[164:167], v[48:51], v[20:23], v[164:167]\n\t"
      "v_mfma_f32_16x16x32_bf16 v[196:199], v[52:55], v[20:23], v[196:199]\n\t"
      "ds_read_b128 v[36:39], v7 offset:56320\n\t"
      "s_waitcnt lgkmcnt(5)\n\t"
      "v_mfma_f32_16x16x32_bf16 v[88:91], v[56:59], v[24:27], v[88:91]\n\t"
      "v_mfma_f32_16x16x32_bf16 v[120:123], v[60:63], v[24:27], v[120:123]\n\t"
      "ds_read_b128 v[40:43], v3 offset:0\n\t"
      "s_waitcnt lgkmcnt(5)\n\t"
      "v_mfma_f32_16x16x32_bf16 v[152:155], v[64:67], v[24:27], v[152:155]\n\t"
      "s_waitcnt lgkmcnt(4)\n\t"
      "v_mfma_f32_16x16x32_bf16 v[184:187], v[68:71], v[24:27], v[184:187]\n\t"
      "ds_read_b128 v[44:47], v3 offset:2048\n\t"
      "ds_read_b128 v[16:19], v7 offset:16384\n\t"
      "s_waitcnt lgkmcnt(5)\n\t"
      "v_mfma_f32_16x16x32_bf16 v[92:95], v[56:59], v[28:31], v[92:95]\n\t"
      "v_mfma_f32_16x16x32_bf16 v[124:127], v[60:63], v[28:31], v[124:127]\n\t"
      "v_mfma_f32_16x16x32_bf16 v[156:159], v[64:67], v[28:31], v[156:159]\n\t"
      "v_mfma_f32_16x16x32_bf16 v[188:191], v[68:71], v[28:31], v[188:191]\n\t"
      "ds_read_b128 v[20:23], v7 offset:17408\n\t"
      "s_waitcnt lgkmcnt(5)\n\t"
      "v_mfma_f32_16x16x32_bf16 v[96:99], v[56:59], v[32:35], v[96:99]\n\t"
      "v_mfma_f32_16x16x32_bf16 v[128:131], v[60:63], v[32:35], v[128:131]\n\t"
      "v_mfma_f32_16x16x32_bf16 v[160:163], v[64:67], v[32:35], v[160:163]\n\t"
      "v_mfma_f32_16x16x32_bf16 v[192:195], v[68:71], v[32:35], v[192:195]\n\t"
      "ds_read_b128 v[24:27], v7 offset:18432\n\t"
      "s_waitcnt lgkmcnt(5)\n\t"
      "v_mfma_f32_16x16x32_bf16 v[100:103], v[56:59], v[36:39], v[100:103]\n\t"
      "v_mfma_f32_16x16x32_bf16 v[132:135], v[60:63], v[36:39], v[132:135]\n\t"
      "v_mfma_f32_16x16x32_bf16 v[164:167], v[64:67], v[36:39], v[164:167]\n\t"
      "v_mfma_f32_16x16x32_bf16 v[196:199], v[68:71], v[36:39], v[196:199]\n\t"
      "ds_read_b128 v[28:31], v7 offset:19456\n\t"
      "s_waitcnt lgkmcnt(3)\n\t"
      "v_mfma_f32_16x16x32_bf16 v[200:203], v[40:43], v[16:19], v[200:203]\n\t"
      "v_cvt_pk_bf16_f32 v88, v88, v92\n\t"
      "v_cvt_pk_bf16_f32 v92, v89, v93\n\t"
      "v_cvt_pk_bf16_f32 v89, v96, v100\n\t"
      "v_cvt_pk_bf16_f32 v93, v97, v101\n\t"
      "buffer_store_dwordx2 v[88:89], v8, s[36:39], s60 offen offset:128 sc1\n\t"
      "buffer_store_dwordx2 v[92:93], v8, s[36:39], s61 offen offset:128 sc1\n\t"
      "v_cvt_pk_bf16_f32 v90, v90, v94\n\t"
      "v_cvt_pk_bf16_f32 v94, v91, v95\n\t"
      "v_cvt_pk_bf16_f32 v91, v98, v102\n\t"
      "v_cvt_pk_bf16_f32 v95, v99, v103\n\t"
      "buffer_store_dwordx2 v[90:91], v8, s[36:39], s62 offen offset:128 sc1\n\t"
      "buffer_store_dwordx2 v[94:95], v8, s[36:39], s63 offen offset:128 sc1\n\t"
      "v_cvt_pk_bf16_f32 v120, v120, v124\n\t"
      "v_cvt_pk_bf16_f32 v124, v121, v125\n\t"
      "v_cvt_pk_bf16_f32 v121, v128, v132\n\t"
      "v_cvt_pk_bf16_f32 v125, v129, v133\n\t"
      "buffer_store_dwordx2 v[120:121], v8, s[36:39], s64 offen offset:128 sc1\n\t"
      "buffer_store_dwordx2 v[124:125], v8, s[36:39], s65 offen offset:128 sc1\n\t"
      "v_cvt_pk_bf16_f32 v122, v122, v126\n\t"
      "v_cvt_pk_bf16_f32 v126, v123, v127\n\t"
      "v_cvt_pk_bf16_f32 v123, v130, v134\n\t"
      "v_cvt_pk_bf16_f32 v127, v131, v135\n\t"
      "buffer_store_dwordx2 v[122:123], v8, s[36:39], s66 offen offset:128 sc1\n\t"
      "buffer_store_dwordx2 v[126:127], v8, s[36:39], s67 offen offset:128 sc1\n\t"
      "v_cvt_pk_bf16_f32 v152, v152, v156\n\t"
      "v_cvt_pk_bf16_f32 v156, v153, v157\n\t"
      "v_cvt_pk_bf16_f32 v153, v160, v164\n\t"
      "v_cvt_pk_bf16_f32 v157, v161, v165\n\t"
      "buffer_store_dwordx2 v[152:153], v9, s[36:39], s60 offen offset:128 sc1\n\t"
      "buffer_store_dwordx2 v[156:157], v9, s[36:39], s61 offen offset:128 sc1\n\t"
      "v_cvt_pk_bf16_f32 v154, v154, v158\n\t"
      "v_cvt_pk_bf16_f32 v158, v155, v159\n\t"
      "v_cvt_pk_bf16_f32 v155, v162, v166\n\t"
      "v_cvt_pk_bf16_f32 v159, v163, v167\n\t"
      "buffer_store_dwordx2 v[154:155], v9, s[36:39], s62 offen offset:128 sc1\n\t"
      "buffer_store_dwordx2 v[158:159], v9, s[36:39], s63 offen offset:128 sc1\n\t"
      "v_cvt_pk_bf16_f32 v184, v184, v188\n\t"
      "v_cvt_pk_bf16_f32 v188, v185, v189\n\t"
      "v_cvt_pk_bf16_f32 v185, v192, v196\n\t"
      "v_cvt_pk_bf16_f32 v189, v193, v197\n\t"
      "buffer_store_dwordx2 v[184:185], v9, s[36:39], s64 offen offset:128 sc1\n\t"
      "buffer_store_dwordx2 v[188:189], v9, s[36:39], s65 offen offset:128 sc1\n\t"
      "v_cvt_pk_bf16_f32 v186, v186, v190\n\t"
      "v_cvt_pk_bf16_f32 v190, v187, v191\n\t"
      "v_cvt_pk_bf16_f32 v187, v194, v198\n\t"
      "v_cvt_pk_bf16_f32 v191, v195, v199\n\t"
      "buffer_store_dwordx2 v[186:187], v9, s[36:39], s66 offen offset:128 sc1\n\t"
      "buffer_store_dwordx2 v[190:191], v9, s[36:39], s67 offen offset:128 sc1\n\t"
      "v_mfma_f32_16x16x32_bf16 v[208:211], v[44:47], v[16:19], v[208:211]\n\t"
      "ds_read_b128 v[56:59], v4 offset:0\n\t"
      "ds_read_b128 v[32:35], v7 offset:36864\n\t"
      "s_waitcnt lgkmcnt(4)\n\t"
      "v_mfma_f32_16x16x32_bf16 v[204:207], v[40:43], v[20:23], v[204:207]\n\t"
      "v_mfma_f32_16x16x32_bf16 v[212:215], v[44:47], v[20:23], v[212:215]\n\t"
      "ds_read_b128 v[60:63], v4 offset:2048\n\t"
      "ds_read_b128 v[36:39], v7 offset:37888\n\t"
      "s_waitcnt lgkmcnt(3)\n\t"
      "v_mfma_f32_16x16x32_bf16 v[200:203], v[56:59], v[24:27], v[200:203]\n\t"
      "s_waitcnt lgkmcnt(1)\n\t"
      "v_mfma_f32_16x16x32_bf16 v[208:211], v[60:63], v[24:27], v[208:211]\n\t"
      "ds_read_b128 v[40:43], v3 offset:32768\n\t"
      "ds_read_b128 v[16:19], v7 offset:38912\n\t"
      "v_mfma_f32_16x16x32_bf16 v[204:207], v[56:59], v[28:31], v[204:207]\n\t"
      "v_mfma_f32_16x16x32_bf16 v[212:215], v[60:63], v[28:31], v[212:215]\n\t"
      "ds_read_b128 v[44:47], v3 offset:34816\n\t"
      "ds_read_b128 v[20:23], v7 offset:39936\n\t"
      "s_waitcnt lgkmcnt(3)\n\t"
      "v_mfma_f32_16x16x32_bf16 v[200:203], v[40:43], v[32:35], v[200:203]\n\t"
      "s_waitcnt lgkmcnt(1)\n\t"
      "v_mfma_f32_16x16x32_bf16 v[208:211], v[44:47], v[32:35], v[208:211]\n\t"
      "ds_read_b128 v[56:59], v4 offset:32768\n\t"
      "ds_read_b128 v[24:27], v7 offset:57344\n\t"
      "v_mfma_f32_16x16x32_bf16 v[204:207], v[40:43], v[36:39], v[204:207]\n\t"
      "v_mfma_f32_16x16x32_bf16 v[212:215], v[44:47], v[36:39], v[212:215]\n\t"
      "ds_read_b128 v[60:63], v4 offset:34816\n\t"
      "ds_read_b128 v[28:31], v7 offset:58368\n\t"
      "s_waitcnt lgkmcnt(3)\n\t"
      "v_mfma_f32_16x16x32_bf16 v[200:203], v[56:59], v[16:19], v[200:203]\n\t"
      "s_waitcnt lgkmcnt(1)\n\t"
      "v_mfma_f32_16x16x32_bf16 v[208:211], v[60:63], v[16:19], v[208:211]\n\t"
      "ds_read_b128 v[40:43], v5 offset:0\n\t"
      "ds_read_b128 v[32:35], v7 offset:59392\n\t"
      "v_mfma_f32_16x16x32_bf16 v[204:207], v[56:59], v[20:23], v[204:207]\n\t"
      "v_mfma_f32_16x16x32_bf16 v[212:215], v[60:63], v[20:23], v[212:215]\n\t"
      "ds_read_b128 v[44:47], v5 offset:2048\n\t"
      "ds_read_b128 v[36:39], v7 offset:60416\n\t"
      "s_waitcnt lgkmcnt(3)\n\t"
      "v_mfma_f32_16x16x32_bf16 v[200:203], v[40:43], v[24:27], v[200:203]\n\t"
      "s_waitcnt lgkmcnt(1)\n\t"
      "v_mfma_f32_16x16x32_bf16 v[208:211], v[44:47], v[24:27], v[208:211]\n\t"
      "ds_read_b128 v[56:59], v6 offset:0\n\t"
      "v_mfma_f32_16x16x32_bf16 v[204:207], v[40:43], v[28:31], v[204:207]\n\t"
      "v_mfma_f32_16x16x32_bf16 v[212:215], v[44:47], v[28:31], v[212:215]\n\t"
      "ds_read_b128 v[60:63], v6 offset:2048\n\t"
      "s_waitcnt lgkmcnt(1)\n\t"
      "v_mfma_f32_16x16x32_bf16 v[200:203], v[56:59], v[32:35], v[200:203]\n\t"
      "s_waitcnt lgkmcnt(0)\n\t"
      "v_mfma_f32_16x16x32_bf16 v[208:211], v[60:63], v[32:35], v[208:211]\n\t"
      "v_mfma_f32_16x16x32_bf16 v[204:207], v[56:59], v[36:39], v[204:207]\n\t"
      "v_mfma_f32_16x16x32_bf16 v[212:215], v[60:63], v[36:39], v[212:215]\n\t"
      "s_nop 6\n\t"
      "v_cvt_pk_bf16_f32 v200, v200, v204\n\t"
      "buffer_store_dword v200, v10, s[36:39], s60 offen sc1\n\t"
      "v_cvt_pk_bf16_f32 v201, v201, v205\n\t"
      "buffer_store_dword v201, v10, s[36:39], s61 offen sc1\n\t"
      "v_cvt_pk_bf16_f32 v202, v202, v206\n\t"
      "buffer_store_dword v202, v10, s[36:39], s62 offen sc1\n\t"
      "v_cvt_pk_bf16_f32 v203, v203, v207\n\t"
      "buffer_store_dword v203, v10, s[36:39], s63 offen sc1\n\t"
      "v_cvt_pk_bf16_f32 v208, v208, v212\n\t"
      "buffer_store_dword v208, v10, s[36:39], s64 offen sc1\n\t"
      "v_cvt_pk_bf16_f32 v209, v209, v213\n\t"
      "buffer_store_dword v209, v10, s[36:39], s65 offen sc1\n\t"
      "v_cvt_pk_bf16_f32 v210, v210, v214\n\t"
      "buffer_store_dword v210, v10, s[36:39], s66 offen sc1\n\t"
      "v_cvt_pk_bf16_f32 v211, v211, v215\n\t"
      "buffer_store_dword v211, v10, s[36:39], s67 offen sc1\n\t"
      "LE_%=:\n\t"
      :
      : [tid] "{v0}"(tid), [ka] "s"(ka), [bid] "s"(bid), [lds] "s"(lds)
      : "memory", "scc", "vcc", "v1", "v2", "v3", "v4", "v5", "v6", "v7", "v8", "v9", "v10", "v11", "v12", "v13", "v14", "v15", "v16", "v17", "v18", "v19", "v20", "v21", "v22", "v23", "v24", "v25", "v26", "v27", "v28", "v29", "v30", "v31", "v32", "v33", "v34", "v35", "v36", "v37", "v38", "v39", "v40", "v41", "v42", "v43", "v44", "v45", "v46", "v47", "v48", "v49", "v50", "v51", "v52", "v53", "v54", "v55", "v56", "v57", "v58", "v59", "v60", "v61", "v62", "v63", "v64", "v65", "v66", "v67", "v68", "v69", "v70", "v71", "v72", "v73", "v74", "v75", "v76", "v77", "v78", "v79", "v80", "v81", "v82", "v83", "v84", "v85", "v86", "v87", "v88", "v89", "v90", "v91", "v92", "v93", "v94", "v95", "v96", "v97", "v98", "v99", "v100", "v101", "v102", "v103", "v104", "v105", "v106", "v107", "v108", "v109", "v110", "v111", "v112", "v113", "v114", "v115", "v116", "v117", "v118", "v119", "v120", "v121", "v122", "v123", "v124", "v125", "v126", "v127", "v128", "v129", "v130", "v131", "v132", "v133", "v134", "v135", "v136", "v137", "v138", "v139", "v140", "v141", "v142", "v143", "v144", "v145", "v146", "v147", "v148", "v149", "v150", "v151", "v152", "v153", "v154", "v155", "v156", "v157", "v158", "v159", "v160", "v161", "v162", "v163", "v164", "v165", "v166", "v167", "v168", "v169", "v170", "v171", "v172", "v173", "v174", "v175", "v176", "v177", "v178", "v179", "v180", "v181", "v182", "v183", "v184", "v185", "v186", "v187", "v188", "v189", "v190", "v191", "v192", "v193", "v194", "v195", "v196", "v197", "v198", "v199", "v200", "v201", "v202", "v203", "v204", "v205", "v206", "v207", "v208", "v209", "v210", "v211", "v212", "v213", "v214", "v215", "v216", "v217", "v218", "v219", "v220", "v221", "v222", "v223", "v224", "v225", "v226", "v227", "v228", "v229", "v230", "v231", "v232", "v233", "v234", "v235", "v236", "v237", "v238", "v239", "v240", "v241", "v242", "v243", "v244", "v245", "v246", "v247", "v248", "v249", "v250", "v251", "v252", "v253", "v254", "v255", "s4", "s5", "s6", "s7", "s8", "s9", "s10", "s11", "s12", "s13", "s14", "s15", "s16", "s17", "s18", "s19", "s20", "s21", "s22", "s23", "s24", "s25", "s26", "s27", "s28", "s29", "s30", "s31", "s36", "s37", "s38", "s39", "s40", "s41", "s42", "s43", "s44", "s45", "s46", "s47", "s48", "s49", "s50", "s51", "s52", "s53", "s54", "s55", "s56", "s57", "s58", "s59", "s60", "s61", "s62", "s63", "s64", "s65", "s66", "s67", "s68", "s69", "s70", "s71", "s72", "s73", "s74", "s75", "s76", "s77", "s78", "s79");
}

extern "C" void kernel_launch(void* const* d_in, const int* in_sizes, int n_in, void* d_out, int out_size, void* d_ws,
                              size_t ws_size, hipStream_t stream) {
  w4a16_gemm_kernel<<<256, 512, 0, stream>>>(d_in[0], d_in[1], d_in[2], d_in[3], d_out);
}
```
